```python
import torch

def _softmax(x, axis=-1):
    return torch.softmax(x, dim=axis)
def _gelu(x):
    return torch.nn.functional.gelu(x, approximate="tanh")
def _silu(x):
    return torch.nn.functional.silu(x)
def _sigmoid(x):
    return torch.sigmoid(x)
def _layernorm(x, axis=-1, eps=1e-5):
    m = x.mean(dim=axis, keepdim=True)
    v = x.var(dim=axis, keepdim=True, unbiased=False)
    return (x - m) / torch.sqrt(v + eps)
def _scatter_add(tgt, idx, src, axis=0):
    return tgt.clone().index_add_(axis, idx, src)
def _segment_sum(src, seg, n):
    out = torch.zeros((n,) + src.shape[1:], dtype=src.dtype)
    return out.index_add_(0, seg, src)
def _pad(x, pw):
    flat = tuple(v for pair in reversed(pw) for v in pair)
    return torch.nn.functional.pad(x, flat)

kernel_name = 'concat_heads__spectral_gfnet__mha_block__prompt'

def setup_inputs():

    _w = (lambda a: (lambda t: t.to(torch.bfloat16) if t.is_floating_point() else t)(torch.as_tensor(a)))
    def _A_setup():
        B, S, D = 1, 2048, 448
        Df = D // 2 + 1
        return {
            "x": _w((lambda *s: torch.randn(*s, dtype=torch.bfloat16) * 0.05)(B, S, D)),
            "w_r": _w((lambda *s: torch.randn(*s, dtype=torch.bfloat16) * 6.4)(S, Df) * 0.02),
            "w_i": _w((lambda *s: torch.randn(*s, dtype=torch.bfloat16) * 6.4)(S, Df) * 0.02),
        }
    _A_in = _A_setup()

    def _B_setup():
        B, S, D, H = 1, 2048, 448, 16
        return {
            "x": _w((lambda *s: torch.randn(*s, dtype=torch.bfloat16) * 0.2)(B, S, D)),
            "wqkv": _w((lambda *s: torch.randn(*s, dtype=torch.bfloat16) * 0.2)(D, 3 * D)),
            "wo": _w((lambda *s: torch.randn(*s, dtype=torch.bfloat16) * 0.05)(D, D)),
        }
    _B_in = _B_setup()

    return {
        "A_x": _A_in["x"],
        "A_w_r": _A_in["w_r"],
        "A_w_i": _A_in["w_i"],
        "B_x": _B_in["x"],
        "B_wqkv": _B_in["wqkv"],
        "B_wo": _B_in["wo"],
    }

def reference(A_x, A_w_r, A_w_i, B_x, B_wqkv, B_wo):
    def _A_ref(x, w_r, w_i):
        B, S, D = x.shape
        X = (lambda x, axes=(-2,-1): torch.fft.rfft2(x.float(), dim=axes))(x, axes=(-2, -1))
        W = w_r + 1j * w_i
        X = X * W
        y = (lambda x, s, axes=(-2,-1): torch.fft.irfft2(x, s=s, dim=axes).to(torch.bfloat16))(X, (S, D), axes=(-2, -1))
        return y

    def _B_ref(x, wqkv, wo):
        B, S, D = x.shape
        H = 16
        Dh = D // H
        qkv = (lambda x, *d: x.permute(*d))((x @ wqkv).reshape(B, S, 3, H, Dh), 2, 0, 3, 1, 4)
        q, k, v = qkv[0], qkv[1], qkv[2]
        scores = torch.einsum("bhqd,bhkd->bhqk", q, k) * (Dh ** -0.5)
        p = _softmax(scores, axis=-1)
        o = torch.einsum("bhqk,bhkd->bhqd", p, v)
        return (lambda x, *d: x.permute(*d))(o, 0, 2, 1, 3).reshape(B, S, D) @ wo

    a = _A_ref(A_x, A_w_r, A_w_i)
    b = _B_ref(B_x, B_wqkv, B_wo)
    return torch.concatenate([a, b], axis=-1)
```

```cpp
#include <hip/hip_runtime.h>
#include <hip/hip_bf16.h>
#include <stdint.h>
typedef unsigned short u16;
__device__ __forceinline__ float bf2f(u16 v){ return __uint_as_float(((unsigned)v)<<16);}
__device__ __forceinline__ u16 f2bf(float f){ __hip_bfloat16 b = __float2bfloat16(f); return *(u16*)&b; }
constexpr int S=2048, D=448, DF=225, H=16, DH=28;

__global__ void k_qkv(const u16* x, const u16* w, float* qkv){
  int n = blockIdx.x*64+threadIdx.x; int s = blockIdx.y;
  if(n>=3*D) return;
  float acc=0; for(int k=0;k<D;k++) acc += bf2f(x[s*D+k])*bf2f(w[k*3*D+n]);
  qkv[s*3*D+n] = bf2f(f2bf(acc));
}
__global__ void k_attn(const float* qkv, float* o){
  __shared__ float sc[S]; __shared__ float red[256];
  int q = blockIdx.x, h = blockIdx.y, t = threadIdx.x;
  float m=-1e30f;
  for(int k=t;k<S;k+=256){ float a=0; for(int d=0;d<DH;d++) a+= qkv[q*3*D+h*DH+d]*qkv[k*3*D+D+h*DH+d]; a*=0.1889822365f; sc[k]=a; m=fmaxf(m,a);}
  red[t]=m; __syncthreads(); for(int s=128;s>0;s>>=1){ if(t<s) red[t]=fmaxf(red[t],red[t+s]); __syncthreads(); }
  m=red[0]; __syncthreads();
  float l=0; for(int k=t;k<S;k+=256){ float p=expf(sc[k]-m); sc[k]=p; l+=p; }
  red[t]=l; __syncthreads(); for(int s=128;s>0;s>>=1){ if(t<s) red[t]+=red[t+s]; __syncthreads(); }
  l=red[0];
  if(t<DH){ float a=0; for(int k=0;k<S;k++) a+= sc[k]*qkv[k*3*D+2*D+h*DH+t]; o[q*D+h*DH+t]=bf2f(f2bf(a/l)); }
}
__global__ void k_proj(const float* o, const u16* wo, u16* out){
  int n = blockIdx.x*64+threadIdx.x; int s = blockIdx.y;
  float acc=0; for(int k=0;k<D;k++) acc += o[s*D+k]*bf2f(wo[k*D+n]);
  out[s*2*D+D+n]=f2bf(acc);
}
__global__ void k_a1(const u16* x, float2* X1){
  int k = threadIdx.x; int s = blockIdx.x; if(k>=DF) return;
  float re=0, im=0; for(int d=0;d<D;d++){ int idx=(k*d)%D; float sn,cs; sincospif(-2.0f*idx/D,&sn,&cs); float v=bf2f(x[s*D+d]); re+=v*cs; im+=v*sn; }
  X1[s*DF+k]=make_float2(re,im);
}
__global__ void k_a2(const float2* X1, const u16* wr, const u16* wi, float2* Y){
  __shared__ float2 tw[S];
  int k = threadIdx.x; int ks = blockIdx.x;
  for(int i=k;i<S;i+=256){ float sn,cs; sincospif(-2.0f*i/S,&sn,&cs); tw[i]=make_float2(cs,sn);} __syncthreads();
  if(k>=DF) return;
  float re=0, im=0; for(int s=0;s<S;s++){ float2 t=tw[(ks*s)&(S-1)]; float2 v=X1[s*DF+k]; re+=v.x*t.x-v.y*t.y; im+=v.x*t.y+v.y*t.x; }
  float a=bf2f(wr[ks*DF+k]), b=bf2f(wi[ks*DF+k]);
  Y[ks*DF+k]=make_float2(re*a-im*b, re*b+im*a);
}
__global__ void k_a3(const float2* Y, float2* Y1){
  __shared__ float2 tw[S];
  int k = threadIdx.x; int s = blockIdx.x;
  for(int i=k;i<S;i+=256){ float sn,cs; sincospif(2.0f*i/S,&sn,&cs); tw[i]=make_float2(cs,sn);} __syncthreads();
  if(k>=DF) return;
  float re=0, im=0; for(int ks=0;ks<S;ks++){ float2 t=tw[(ks*s)&(S-1)]; float2 v=Y[ks*DF+k]; re+=v.x*t.x-v.y*t.y; im+=v.x*t.y+v.y*t.x; }
  Y1[s*DF+k]=make_float2(re/S,im/S);
}
__global__ void k_a4(const float2* Y1, u16* out){
  int d = threadIdx.x; int s = blockIdx.x; if(d>=D) return;
  float acc = Y1[s*DF].x + ((d&1)?-1.f:1.f)*Y1[s*DF+224].x;
  for(int k=1;k<224;k++){ int idx=(k*d)%D; float sn,cs; sincospif(2.0f*idx/D,&sn,&cs); float2 v=Y1[s*DF+k]; acc += 2.f*(v.x*cs - v.y*sn);}
  out[s*2*D+d]=f2bf(acc/D);
}
extern "C" void kernel_launch(void* const* d_in, const int* in_sizes, int n_in, void* d_out, int out_size, void* d_ws, size_t ws_size, hipStream_t stream){
  const u16* ax=(const u16*)d_in[0]; const u16* wr=(const u16*)d_in[1]; const u16* wi=(const u16*)d_in[2];
  const u16* bx=(const u16*)d_in[3]; const u16* wqkv=(const u16*)d_in[4]; const u16* wo=(const u16*)d_in[5];
  u16* out=(u16*)d_out; char* ws=(char*)d_ws;
  float* qkv=(float*)ws; float* o=(float*)(ws+(16<<20)); float2* X1=(float2*)(ws+(32<<20)); float2* Y=(float2*)(ws+(48<<20)); float2* Y1=(float2*)(ws+(64<<20));
  k_qkv<<<dim3(21,S),64,0,stream>>>(bx,wqkv,qkv);
  k_attn<<<dim3(S,H),256,0,stream>>>(qkv,o);
  k_proj<<<dim3(7,S),64,0,stream>>>(o,wo,out);
  k_a1<<<S,256,0,stream>>>(ax,X1);
  k_a2<<<S,256,0,stream>>>(X1,wr,wi,Y);
  k_a3<<<S,256,0,stream>>>(Y,Y1);
  k_a4<<<S,448,0,stream>>>(Y1,out);
}
```

```cpp
#include <hip/hip_runtime.h>
#include <stdint.h>

typedef unsigned short u16;
typedef short bf16x8 __attribute__((ext_vector_type(8)));
typedef short s16x4 __attribute__((ext_vector_type(4)));
typedef float f32x16 __attribute__((ext_vector_type(16)));
typedef float f32x4 __attribute__((ext_vector_type(4)));
typedef float f32x2 __attribute__((ext_vector_type(2)));
typedef unsigned u32x4 __attribute__((ext_vector_type(4)));
typedef unsigned u32x2 __attribute__((ext_vector_type(2)));
typedef __bf16 bf16x2_t __attribute__((ext_vector_type(2)));
#define LDSP __attribute__((address_space(3)))
#define DI __device__ __forceinline__

constexpr int SEQ = 2048, DM = 448, NH = 16, NF = 225;

constexpr size_t WS_Q  = 0;
constexpr size_t WS_KV = WS_Q + (2u << 20);
constexpr size_t WS_X1 = WS_KV + (4u << 20);
constexpr size_t WS_WF = WS_X1 + (2u << 20);
constexpr size_t WS_Y1 = WS_WF + (2u << 20);
constexpr size_t WS_O  = WS_Y1 + (2u << 20);
constexpr size_t WS_WO = WS_O + (2u << 20);
constexpr size_t WS_G  = WS_WO + (512u << 10);
constexpr size_t WS_T1 = WS_G + (256u << 10);
constexpr size_t WS_T2 = WS_T1 + (32u << 10);
constexpr size_t WS_TW = WS_T2 + (8u << 10);
constexpr size_t WS_END = WS_TW + (32u << 10);

DI unsigned pk2(float lo, float hi) {
  bf16x2_t v = __builtin_convertvector((f32x2){lo, hi}, bf16x2_t);
  return __builtin_bit_cast(unsigned, v);
}
DI float bflo(unsigned u) { return __uint_as_float(u << 16); }
DI float bfhi(unsigned u) { return __uint_as_float(u & 0xffff0000u); }
DI f32x16 mfma32(bf16x8 a, bf16x8 b, f32x16 c) { return __builtin_amdgcn_mfma_f32_32x32x16_bf16(a, b, c, 0, 0, 0); }
DI f32x4 mfma16(bf16x8 a, bf16x8 b, f32x4 c) { return __builtin_amdgcn_mfma_f32_16x16x32_bf16(a, b, c, 0, 0, 0); }
DI bf16x8 asb(u32x4 v) { return __builtin_bit_cast(bf16x8, v); }
DI int crow(int r, int h) { return (r & 3) + 8 * (r >> 2) + 4 * h; }
DI int phi(int rt, int rho) { return 8 * (rho >> 2) + 4 * rt + (rho & 3); }
DI float sinr(float rev) { return __builtin_amdgcn_sinf(rev); }
DI float cosr(float rev) { return __builtin_amdgcn_cosf(rev); }
DI void glds16(const void* g, unsigned lds_base) {
  unsigned sv;
  asm volatile("s_mov_b32 %0, m0\n\ts_mov_b32 m0, %2\n\ts_nop 0\n\tglobal_load_lds_dwordx4 %1, off\n\ts_mov_b32 m0, %0"
               : "=&s"(sv) : "v"(g), "s"(lds_base) : "memory");
}
#define WAITV_BAR(N) asm volatile("s_waitcnt vmcnt(" #N ") lgkmcnt(0)\n\ts_barrier" ::: "memory")
DI bf16x8 ldsr(const LDSP char* p) { return *(const LDSP bf16x8*)p; }
DI s16x4 ldtr(const LDSP char* p) { return __builtin_amdgcn_ds_read_tr16_b64_v4i16((LDSP s16x4*)p); }
DI bf16x8 cat(s16x4 a, s16x4 b) { return __builtin_shufflevector(a, b, 0, 1, 2, 3, 4, 5, 6, 7); }
DI float swapadd(float v) {
  auto r = __builtin_amdgcn_permlane32_swap(__float_as_uint(v), __float_as_uint(v), false, false);
  return __uint_as_float(r[0]) + __uint_as_float(r[1]);
}
DI u32x4 pack8(const f32x16& x, int o) { return (u32x4){pk2(x[o], x[o + 1]), pk2(x[o + 2], x[o + 3]), pk2(x[o + 4], x[o + 5]), pk2(x[o + 6], x[o + 7])}; }

__global__ __launch_bounds__(256, 1) void k1(const u16* __restrict__ ax, const u16* __restrict__ wr, const u16* __restrict__ wi,
                                             const u16* __restrict__ bx, const u16* __restrict__ wqkv, const u16* __restrict__ wo,
                                             char* __restrict__ ws) {
  __shared__ __attribute__((aligned(1024))) char smem[131072];
  const LDSP char* L = (const LDSP char*)smem;
  const unsigned lds0 = (unsigned)(uintptr_t)(LDSP char*)smem;
  const int tid = threadIdx.x, lane = tid & 63, w = __builtin_amdgcn_readfirstlane(tid >> 6);
  const int r32 = lane & 31, h = lane >> 5;
  const int xcd = blockIdx.x & 7, idx = blockIdx.x >> 3;
  const unsigned xsw = (unsigned)((r32 * 128) + (((h ^ (r32 >> 1)) & 7) << 4));

  if (idx < 24) {
    const int rb = (xcd & 3) * 4 + (idx & 3), cg = (xcd >> 2) * 6 + (idx >> 2);
    const bool isv = cg >= 8;
    const int xrow = 32 * w + (lane >> 3);
    const char* xsrc = (const char*)bx + (size_t)(rb * 128 + xrow) * 896 + (((lane & 7) ^ ((xrow >> 1) & 7)) << 4);
    const int c16 = (lane & 15) ^ ((lane >> 4) << 2);
    const int wcol = isv ? 1792 + (cg - 8) * 224 + c16 * 16 : (c16 < 8 ? cg * 112 + c16 * 16 : 896 + cg * 112 + (c16 - 8) * 16);
    const unsigned woff = (unsigned)((16 * w + (lane >> 4)) * 2688 + wcol);
    (void)xsrc;
    auto issue = [&](int c, int slot) {
#pragma unroll
      for (int i = 0; i < 4; ++i) {
        const int row = 32 * w + 8 * i + (lane >> 3);
        const char* s = (const char*)bx + (size_t)(rb * 128 + row) * 896 + c * 128 + (((lane & 7) ^ ((row >> 1) & 7)) << 4);
        glds16(s, lds0 + slot * 32768 + (4 * w + i) * 1024);
      }
#pragma unroll
      for (int i = 0; i < 4; ++i) {
        unsigned o = woff + (unsigned)((64 * c + 4 * i) * 2688);
        o = o > (448u * 2688u - 16u) ? (448u * 2688u - 16u) : o;
        glds16((const char*)wqkv + o, lds0 + slot * 32768 + 16384 + (4 * w + i) * 1024);
      }
    };
    issue(0, 0); issue(1, 1); issue(2, 2); issue(3, 3);
    const int wr2 = w >> 1, wc = w & 1;
    const int o0 = isv ? 112 * wc : 56 * wc, o1 = isv ? 112 * wc + 56 : 128 + 56 * wc;
    const int blk = (lane >> 4) & 1, q = (lane & 15) >> 2, p = lane & 3;
    const int b0 = o0 + 32 * blk + 8 * p, b1 = o1 + 32 * blk + 8 * p;
    const unsigned wa0 = 16384 + (8 * h + q) * 256 + ((((b0 >> 4) ^ (q << 2)) & 15) << 4) + (b0 & 8);
    const unsigned wa1 = 16384 + (8 * h + q) * 256 + ((((b1 >> 4) ^ (q << 2)) & 15) << 4) + (b1 & 8);
    f32x16 acc[2][2] = {};
    auto compute = [&](int slot, bool v) {
#pragma unroll
      for (int kk = 0; kk < 4; ++kk) {
        const LDSP char* xb = L + slot * 32768 + (xsw ^ (kk << 5)) + wr2 * 8192;
        bf16x8 x0 = ldsr(xb), x1 = ldsr(xb + 4096);
        const LDSP char* p0 = L + slot * 32768 + wa0 + kk * 4096;
        const LDSP char* p1 = L + slot * 32768 + wa1 + kk * 4096;
        bf16x8 w0 = cat(ldtr(p0), ldtr(p0 + 1024)), w1 = cat(ldtr(p1), ldtr(p1 + 1024));
        if (v) {
          acc[0][0] = mfma32(x0, w0, acc[0][0]); acc[0][1] = mfma32(x0, w1, acc[0][1]);
          acc[1][0] = mfma32(x1, w0, acc[1][0]); acc[1][1] = mfma32(x1, w1, acc[1][1]);
        } else {
          acc[0][0] = mfma32(w0, x0, acc[0][0]); acc[0][1] = mfma32(w1, x0, acc[0][1]);
          acc[1][0] = mfma32(w0, x1, acc[1][0]); acc[1][1] = mfma32(w1, x1, acc[1][1]);
        }
      }
    };
#define K1LOOP(V)                                   \
    WAITV_BAR(24); compute(0, V);                      \
    WAITV_BAR(16); issue(4, 0); compute(1, V);         \
    WAITV_BAR(16); issue(5, 1); compute(2, V);         \
    WAITV_BAR(16); issue(6, 2); compute(3, V);         \
    WAITV_BAR(16); compute(0, V);                      \
    WAITV_BAR(8); compute(1, V);                       \
    WAITV_BAR(0); compute(2, V);
    if (isv) {
      K1LOOP(true)
#pragma unroll
      for (int i = 0; i < 2; ++i)
#pragma unroll
        for (int j = 0; j < 2; ++j) {
          const int head = (cg - 8) * 4 + 2 * wc + j, kt = rb * 4 + 2 * wr2 + i;
          u32x4 a = pack8(acc[i][j], 0), b = pack8(acc[i][j], 8);
          if (r32 >= 28) { const unsigned f = r32 == 28 ? 0x3f803f80u : 0u; a = (u32x4){f, f, f, f}; b = a; }
          u32x4* dst = (u32x4*)(ws + WS_KV + ((size_t)(head * 64 + kt) * 4 + 2) * 1024) + lane;
          dst[0] = a; dst[64] = b;
        }
    } else {
      K1LOOP(false)
      const float qs = 0.18898223650461362f * 1.4426950408889634f;
#pragma unroll
      for (int i = 0; i < 2; ++i) {
        const int head = cg * 2 + wc, rt = rb * 4 + 2 * wr2 + i;
        f32x16 qv = acc[i][0] * qs, kv = acc[i][1];
        float sd = 0.f;
#pragma unroll
        for (int r = 0; r < 12; ++r) sd += qv[r] * kv[r];
        float s2 = 0.f;
#pragma unroll
        for (int r = 12; r < 16; ++r) s2 += qv[r] * kv[r];
        sd += h ? 0.f : s2;
        sd = swapadd(sd);
        u32x4 q0 = pack8(qv, 0), q1 = pack8(qv, 8), k0 = pack8(kv, 0), k1v = pack8(kv, 8);
        if (h) { q1[2] = pk2(-sd, 0.f); q1[3] = 0u; k1v[2] = 0x00003f80u; k1v[3] = 0u; }
        u32x4* dq = (u32x4*)(ws + WS_Q + (size_t)(head * 64 + rt) * 2048) + lane;
        dq[0] = q0; dq[64] = q1;
        u32x4* dk = (u32x4*)(ws + WS_KV + (size_t)(head * 64 + rt) * 4096) + lane;
        dk[0] = k0; dk[64] = k1v;
      }
    }
    return;
  }
  const int j8 = idx - 24, m = 2 * xcd + (j8 & 1), kg = j8 >> 1, fw = xcd * 8 + j8;
  auto issuex = [&](int c, int slot) {
#pragma unroll
    for (int i = 0; i < 4; ++i) {
      const int rr = 8 * i + (lane >> 3);
      const int s = 32 * (32 * (w >> 1) + rr) + 2 * m + (w & 1);
      const char* src = (const char*)ax + (size_t)s * 896 + c * 128 + (((lane & 7) ^ ((rr >> 1) & 7)) << 4);
      glds16(src, lds0 + slot * 16384 + (4 * w + i) * 1024);
    }
  };
  issuex(0, 0); issuex(1, 1); issuex(2, 2); issuex(3, 3);
  unsigned wv[32];
  if (tid < NF) {
#pragma unroll
    for (int e = 0; e < 32; ++e) {
      const int k2 = phi((e >> 2) & 1, 4 * (e >> 3) + (e & 3));
      const int ks = fw + 64 * k2;
      wv[e] = (unsigned)wr[ks * NF + tid] | ((unsigned)wi[ks * NF + tid] << 16);
    }
  }
  {
    const float kf = (float)(32 * kg + r32) * (1.0f / 448.0f);
    for (int u = w; u < 14; u += 4) {
      float c[16], s[16];
#pragma unroll
      for (int e = 0; e < 16; ++e) {
        const int d = 32 * u + 8 * h + (e & 7) + 16 * (e >> 3);
        const float a = __builtin_amdgcn_fractf((float)d * kf);
        c[e] = cosr(a); s[e] = sinr(a);
      }
      LDSP u32x4* t = (LDSP u32x4*)(smem + 65536 + u * 1024) + lane;
      t[0]        = (u32x4){pk2(c[0], c[2]), pk2(c[4], c[6]), pk2(c[8], c[10]), pk2(c[12], c[14])};
      t[14 * 64]  = (u32x4){pk2(c[1], c[3]), pk2(c[5], c[7]), pk2(c[9], c[11]), pk2(c[13], c[15])};
      t[28 * 64]  = (u32x4){pk2(s[0], s[2]), pk2(s[4], s[6]), pk2(s[8], s[10]), pk2(s[12], s[14])};
      t[42 * 64]  = (u32x4){pk2(s[1], s[3]), pk2(s[5], s[7]), pk2(s[9], s[11]), pk2(s[13], s[15])};
    }
  }
  if (tid < NF) {
    u32x4* dst = (u32x4*)(ws + WS_WF + (size_t)tid * 8192 + fw * 128);
#pragma unroll
    for (int e = 0; e < 8; ++e) dst[e] = (u32x4){wv[4 * e], wv[4 * e + 1], wv[4 * e + 2], wv[4 * e + 3]};
  }
  f32x16 ce = {}, co = {}, se = {}, so = {};
  auto computef = [&](int c, int slot) {
#pragma unroll
    for (int uu = 0; uu < 2; ++uu) {
      const LDSP char* xb = L + slot * 16384 + w * 4096;
      u32x4 a = __builtin_bit_cast(u32x4, ldsr(xb + (xsw ^ (uu << 6)))), b = __builtin_bit_cast(u32x4, ldsr(xb + (xsw ^ (uu << 6) ^ 32)));
      u32x4 ev = {__builtin_amdgcn_perm(a[1], a[0], 0x05040100u), __builtin_amdgcn_perm(a[3], a[2], 0x05040100u),
                  __builtin_amdgcn_perm(b[1], b[0], 0x05040100u), __builtin_amdgcn_perm(b[3], b[2], 0x05040100u)};
      u32x4 od = {__builtin_amdgcn_perm(a[1], a[0], 0x07060302u), __builtin_amdgcn_perm(a[3], a[2], 0x07060302u),
                  __builtin_amdgcn_perm(b[1], b[0], 0x07060302u), __builtin_amdgcn_perm(b[3], b[2], 0x07060302u)};
      const LDSP char* t = L + 65536 + (2 * c + uu) * 1024 + lane * 16;
      ce = mfma32(asb(ev), ldsr(t), ce);
      co = mfma32(asb(od), ldsr(t + 14336), co);
      se = mfma32(asb(ev), ldsr(t + 28672), se);
      so = mfma32(asb(od), ldsr(t + 43008), so);
    }
  };
  WAITV_BAR(0); computef(0, 0);
  WAITV_BAR(0); issuex(4, 0); computef(1, 1);
  WAITV_BAR(4); issuex(5, 1); computef(2, 2);
  WAITV_BAR(8); issuex(6, 2); computef(3, 3);
  WAITV_BAR(8); computef(4, 0);
  WAITV_BAR(4); computef(5, 1);
  WAITV_BAR(0); computef(6, 2);
  {
    const int k = 32 * kg + r32;
    if (k <= 112) {
      const int n2 = 2 * m + (w & 1), a = w >> 1;
      u32x4* d0 = (u32x4*)(ws + WS_X1 + (size_t)k * 8192 + n2 * 256 + a * 128 + h * 16);
      u32x4* d1 = (u32x4*)(ws + WS_X1 + (size_t)(224 - k) * 8192 + n2 * 256 + a * 128 + h * 16);
#pragma unroll
      for (int q4 = 0; q4 < 4; ++q4) {
        const int r = 4 * q4;
        d0[2 * q4] = (u32x4){pk2(ce[r] + co[r], ce[r + 1] + co[r + 1]), pk2(ce[r + 2] + co[r + 2], ce[r + 3] + co[r + 3]),
                             pk2(-se[r] - so[r], -se[r + 1] - so[r + 1]), pk2(-se[r + 2] - so[r + 2], -se[r + 3] - so[r + 3])};
        if (k < 112)
          d1[2 * q4] = (u32x4){pk2(ce[r] - co[r], ce[r + 1] - co[r + 1]), pk2(ce[r + 2] - co[r + 2], ce[r + 3] - co[r + 3]),
                               pk2(se[r] - so[r], se[r + 1] - so[r + 1]), pk2(se[r + 2] - so[r + 2], se[r + 3] - so[r + 3])};
      }
    }
  }
  const int job = fw * 4 + w;
  for (int f = job; f < 448; f += 256) {
    const int nt = f >> 5, ks = f & 31, head = ks >> 1, t = ks & 1;
    unsigned v[8];
#pragma unroll
    for (int j = 0; j < 8; ++j) {
      const int dim = crow(8 * t + j, h);
      v[j] = dim < 28 ? (unsigned)wo[(head * 28 + dim) * DM + nt * 32 + r32] : 0u;
    }
    *((u32x4*)(ws + WS_WO + (size_t)f * 1024) + lane) = (u32x4){v[0] | (v[1] << 16), v[2] | (v[3] << 16), v[4] | (v[5] << 16), v[6] | (v[7] << 16)};
  }
  if (job < 196) {
    const int eo = job / 98, rem = job - eo * 98, dt = rem / 14, ks = rem - dt * 14;
    const int d = dt * 32 + r32;
    const float sc = 1.0f / (448.0f * 2048.0f);
    float v[8];
#pragma unroll
    for (int j = 0; j < 8; ++j) {
      const int pl = 16 * ks + 8 * h + j;
      int kd = (pl & ~1) + eo, part = pl & 1;
      float cf = 2.f * sc;
      if (eo == 0 && pl < 2) { kd = pl ? 224 : 0; part = 0; cf = sc; }
      const float a = __builtin_amdgcn_fractf((float)(kd * d) * (1.0f / 448.0f));
      v[j] = part ? -cf * sinr(a) : cf * cosr(a);
    }
    *((u32x4*)(ws + WS_G + (size_t)job * 1024) + lane) = (u32x4){pk2(v[0], v[1]), pk2(v[2], v[3]), pk2(v[4], v[5]), pk2(v[6], v[7])};
  } else if (job < 228) {
    const int f = job - 196, ww = f >> 3, out = (f >> 2) & 1, wp = f & 3;
    const int a = 16 * ww + (lane & 15), g = lane >> 4;
    float v[8];
#pragma unroll
    for (int j = 0; j < 8; ++j) {
      const int b = 16 * wp + 4 * g + (j & 3);
      const float an = (float)((a * b) & 63) * (1.0f / 64.0f);
      const bool hi = j >= 4;
      v[j] = (hi == (out != 0)) ? cosr(an) : (out ? -sinr(an) : sinr(an));
    }
    *((u32x4*)(ws + WS_T1 + (size_t)f * 1024) + lane) = (u32x4){pk2(v[0], v[1]), pk2(v[2], v[3]), pk2(v[4], v[5]), pk2(v[6], v[7])};
  } else if (job < 236) {
    const int f = job - 228, out = f >> 2, ta = (f >> 1) & 1, tb = f & 1;
    const int a = phi(ta, lane & 15), g = lane >> 4;
    float v[8];
#pragma unroll
    for (int j = 0; j < 8; ++j) {
      const int b = phi(tb, 4 * g + (j & 3));
      const float an = (float)((a * b) & 31) * (1.0f / 32.0f);
      const bool hi = j >= 4;
      v[j] = (hi == (out != 0)) ? cosr(an) : (out ? -sinr(an) : sinr(an));
    }
    *((u32x4*)(ws + WS_T2 + (size_t)f * 1024) + lane) = (u32x4){pk2(v[0], v[1]), pk2(v[2], v[3]), pk2(v[4], v[5]), pk2(v[6], v[7])};
  } else if (job < 244) {
    const int f = job - 236, dir = f >> 2, ww = f & 3, g = lane >> 4;
    f32x2* dst = (f32x2*)(ws + WS_TW + (size_t)f * 4096 + lane * 64);
#pragma unroll
    for (int e = 0; e < 8; ++e) {
      const int rt = e >> 2, r = e & 3;
      const int n2 = dir ? phi(rt, lane & 15) : phi(rt, 4 * g + r);
      const int k1 = dir ? 16 * ww + 4 * g + r : 16 * ww + (lane & 15);
      const float an = (float)(n2 * k1) * (1.0f / 2048.0f);
      dst[e] = (f32x2){cosr(an), sinr(an)};
    }
  }
}

constexpr int K2_EX = 131072;
constexpr int K2_LDS = K2_EX + 8192 + 64;

template <bool SAFE>
DI void attn_loop(const char* kvsrc, unsigned lds0, const LDSP char* L, int w, int lane, bf16x8 q0, bf16x8 q1, float msub, f32x16& o) {
  const int par = w >> 2;
  const unsigned toff = par * 16384 + lane * 16;
  const f32x16 zero = {};
  WAITV_BAR(8);
  const LDSP char* cur = L + toff;
  bf16x8 ka = ldsr(cur), kb = ldsr(cur + 1024);
  f32x16 sA = mfma32(ka, q0, zero); sA = mfma32(kb, q1, sA);
  ka = ldsr(cur + 4096); kb = ldsr(cur + 4096 + 1024);
  bf16x8 va = ldsr(cur + 2048), vb = ldsr(cur + 3072);
  f32x16 sB;
#define STEP(SC, SN, KP, VP)                                                        \
  {                                                                                  \
    SN = mfma32(ka, q0, zero); SN = mfma32(kb, q1, SN);                              \
    ka = ldsr(KP); kb = ldsr((KP) + 1024);                                           \
    bf16x8 vna = ldsr(VP), vnb = ldsr((VP) + 1024);                                  \
    _Pragma("unroll") for (int r = 0; r < 16; ++r) SC[r] = __builtin_amdgcn_exp2f(SAFE ? SC[r] - msub : SC[r]); \
    o = mfma32(va, asb(pack8(SC, 0)), o); o = mfma32(vb, asb(pack8(SC, 8)), o);      \
    va = vna; vb = vnb;                                                              \
  }
  for (int st = 0; st < 8; ++st) {
    const LDSP char* nxt = L + toff + ((st + 1) & 3) * 32768;
    STEP(sA, sB, cur + 8192, cur + 4096 + 2048)
    STEP(sB, sA, cur + 12288, cur + 8192 + 2048)
    STEP(sA, sB, nxt, cur + 12288 + 2048)
    if (st < 5) { WAITV_BAR(4); } else { WAITV_BAR(0); }
    if (st < 4) {
#pragma unroll
      for (int i = 0; i < 4; ++i) glds16(kvsrc + (st + 4) * 32768 + i * 1024, lds0 + st * 32768 + (4 * w + i) * 1024);
    }
    STEP(sB, sA, nxt + 4096, nxt + 2048)
    cur = nxt;
  }
#undef STEP
}

__global__ __launch_bounds__(512, 2) void k2(char* __restrict__ ws) {
  __shared__ __attribute__((aligned(1024))) char smem[K2_LDS];
  const LDSP char* L = (const LDSP char*)smem;
  const unsigned lds0 = (unsigned)(uintptr_t)(LDSP char*)smem;
  const int tid = threadIdx.x, lane = tid & 63, w = __builtin_amdgcn_readfirstlane(tid >> 6);
  const int h = lane >> 5;
  const int xcd = blockIdx.x & 7, j = blockIdx.x >> 3, head = 2 * xcd + (j & 1), qt = (j >> 1) * 4 + (w & 3);
  const int kd = blockIdx.x;
  const u32x4* qsrc = (const u32x4*)(ws + WS_Q + (size_t)(head * 64 + qt) * 2048) + lane;
  u32x4 q0 = qsrc[0], q1 = qsrc[64];
  const char* kvsrc = ws + WS_KV + (size_t)head * 262144 + w * 4096 + lane * 16;
  const bool dofft = kd < NF && w < 4;
  if (tid == 0) *(LDSP unsigned*)(smem + K2_EX + 8192) = 0u;

  const int l16 = lane & 15, g = lane >> 4;
  bf16x8 t1[2][4];
  if (dofft) {
    bf16x8 xf[2][4], t2[2][2][2];
    f32x4 tw1[4], tw2[4];
    u32x4 wf[2];
#pragma unroll
    for (int rt = 0; rt < 2; ++rt)
#pragma unroll
      for (int wp = 0; wp < 4; ++wp) xf[rt][wp] = *(const bf16x8*)(ws + WS_X1 + (size_t)kd * 8192 + phi(rt, l16) * 256 + wp * 64 + g * 16);
#pragma unroll
    for (int o = 0; o < 2; ++o)
#pragma unroll
      for (int wp = 0; wp < 4; ++wp) t1[o][wp] = *(const bf16x8*)(ws + WS_T1 + (w * 8 + o * 4 + wp) * 1024 + lane * 16);
#pragma unroll
    for (int f = 0; f < 8; ++f) t2[f >> 2][(f >> 1) & 1][f & 1] = *(const bf16x8*)(ws + WS_T2 + f * 1024 + lane * 16);
#pragma unroll
    for (int e = 0; e < 4; ++e) { tw1[e] = *(const f32x4*)(ws + WS_TW + w * 4096 + lane * 64 + e * 16); tw2[e] = *(const f32x4*)(ws + WS_TW + (4 + w) * 4096 + lane * 64 + e * 16); }
#pragma unroll
    for (int e = 0; e < 2; ++e) wf[e] = *(const u32x4*)(ws + WS_WF + (size_t)kd * 8192 + (16 * w + l16) * 128 + g * 32 + e * 16);
#pragma unroll
    for (int st = 0; st < 4; ++st)
#pragma unroll
      for (int i = 0; i < 4; ++i) glds16(kvsrc + st * 32768 + i * 1024, lds0 + st * 32768 + (4 * w + i) * 1024);
    const f32x4 z4 = {};
    bf16x8 zf[2];
#pragma unroll
    for (int rt = 0; rt < 2; ++rt) {
      f32x4 zr = z4, zi = z4;
#pragma unroll
      for (int wp = 0; wp < 4; ++wp) { zr = mfma16(xf[rt][wp], t1[0][wp], zr); zi = mfma16(xf[rt][wp], t1[1][wp], zi); }
      float a[4], b[4];
#pragma unroll
      for (int r = 0; r < 4; ++r) {
        const float c = tw1[2 * rt + (r >> 1)][2 * (r & 1)], s = tw1[2 * rt + (r >> 1)][2 * (r & 1) + 1];
        a[r] = zr[r] * c + zi[r] * s; b[r] = zi[r] * c - zr[r] * s;
      }
      zf[rt] = asb((u32x4){pk2(a[0], a[1]), pk2(a[2], a[3]), pk2(b[0], b[1]), pk2(b[2], b[3])});
    }
    bf16x8 yf[2];
#pragma unroll
    for (int ta = 0; ta < 2; ++ta) {
      f32x4 yr = mfma16(t2[0][ta][0], zf[0], z4), yi = mfma16(t2[1][ta][0], zf[0], z4);
      yr = mfma16(t2[0][ta][1], zf[1], yr); yi = mfma16(t2[1][ta][1], zf[1], yi);
      float a[4], b[4];
#pragma unroll
      for (int r = 0; r < 4; ++r) {
        const float c = bflo(wf[ta][r]), s = bfhi(wf[ta][r]);
        a[r] = yr[r] * c - yi[r] * s; b[r] = yr[r] * s + yi[r] * c;
      }
      yf[ta] = asb((u32x4){pk2(b[0], b[1]), pk2(b[2], b[3]), pk2(a[0], a[1]), pk2(a[2], a[3])});
    }
#pragma unroll
    for (int rt = 0; rt < 2; ++rt) {
      f32x4 ur = mfma16(yf[0], t2[1][rt][0], z4), ui = mfma16(yf[0], t2[0][rt][0], z4);
      ur = mfma16(yf[1], t2[1][rt][1], ur); ui = mfma16(yf[1], t2[0][rt][1], ui);
      float a[4], b[4];
#pragma unroll
      for (int r = 0; r < 4; ++r) {
        const float c = tw2[2 * rt + (r >> 1)][2 * (r & 1)], s = tw2[2 * rt + (r >> 1)][2 * (r & 1) + 1];
        a[r] = ur[r] * c - ui[r] * s; b[r] = ui[r] * c + ur[r] * s;
      }
      *((LDSP u32x4*)(smem + K2_EX + (w * 2 + rt) * 1024) + lane) = (u32x4){pk2(b[0], b[1]), pk2(b[2], b[3]), pk2(a[0], a[1]), pk2(a[2], a[3])};
    }
  } else {
#pragma unroll
    for (int st = 0; st < 4; ++st)
#pragma unroll
      for (int i = 0; i < 4; ++i) glds16(kvsrc + st * 32768 + i * 1024, lds0 + st * 32768 + (4 * w + i) * 1024);
  }
  asm volatile("s_waitcnt lgkmcnt(0)\n\ts_barrier" ::: "memory");
  if (dofft) {
    const f32x4 z4 = {};
    f32x4 orr[2], oi[2];
#pragma unroll
    for (int rt = 0; rt < 2; ++rt) {
      orr[rt] = z4; oi[rt] = z4;
#pragma unroll
      for (int wp = 0; wp < 4; ++wp) {
        bf16x8 u = ldsr(L + K2_EX + (wp * 2 + rt) * 1024 + lane * 16);
        orr[rt] = mfma16(u, t1[1][wp], orr[rt]); oi[rt] = mfma16(u, t1[0][wp], oi[rt]);
      }
    }
    const int pre = (kd & 1) ? 223 + kd : (kd == 224 ? 1 : kd);
    char* dst = ws + WS_Y1 + (size_t)pre * 4096 + (16 * w + l16) * 64 + g * 16;
    *(u32x4*)dst = (u32x4){pk2(orr[0][0], orr[0][1]), pk2(orr[0][2], orr[0][3]), pk2(orr[1][0], orr[1][1]), pk2(orr[1][2], orr[1][3])};
    if (kd != 0 && kd != 224)
      *(u32x4*)(dst + 4096) = (u32x4){pk2(oi[0][0], oi[0][1]), pk2(oi[0][2], oi[0][3]), pk2(oi[1][0], oi[1][1]), pk2(oi[1][2], oi[1][3])};
  }
  f32x16 o = {};
  asm volatile("s_waitcnt vmcnt(0)" ::: "memory");
  attn_loop<false>(kvsrc, lds0, L, w, lane, asb(q0), asb(q1), 0.f, o);
  for (int pass = 0;; ++pass) {
    asm volatile("s_waitcnt lgkmcnt(0)\n\ts_barrier" ::: "memory");
    LDSP f32x4* ex = (LDSP f32x4*)(smem + (w & 3) * 8192) + lane;
    const int par = w >> 2;
    const float lme = o[12];
    if (par == 0) { ex[0] = (f32x4){o[8], o[9], o[10], o[11]}; ex[64] = (f32x4){o[12], o[13], o[14], o[15]}; }
    else { ex[128] = (f32x4){o[0], o[1], o[2], o[3]}; ex[192] = (f32x4){o[4], o[5], o[6], o[7]}; ((LDSP float*)(smem + (w & 3) * 8192 + 4096))[lane] = lme; }
    asm volatile("s_waitcnt lgkmcnt(0)\n\ts_barrier" ::: "memory");
    f32x4 a, b; float l;
    if (par == 0) {
      a = ex[128]; b = ex[192]; l = lme + ((LDSP float*)(smem + (w & 3) * 8192 + 4096))[lane];
      a += (f32x4){o[0], o[1], o[2], o[3]}; b += (f32x4){o[4], o[5], o[6], o[7]};
    } else {
      a = ex[0]; b = ex[64];
      a += (f32x4){o[8], o[9], o[10], o[11]}; b += (f32x4){o[12], o[13], o[14], o[15]};
      l = b[0];
    }
    l = swapadd(h ? l : 0.f);
    const float rl = __builtin_amdgcn_rcpf(l);
    a *= rl; b *= rl;
    if (par && h) b = (f32x4){0.f, 0.f, 0.f, 0.f};
    if (pass == 0) {
      const float mx = fmaxf(fmaxf(fmaxf(fabsf(a[0]), fabsf(a[1])), fmaxf(fabsf(a[2]), fabsf(a[3]))), fmaxf(fmaxf(fabsf(b[0]), fabsf(b[1])), fmaxf(fabsf(b[2]), fabsf(b[3]))));
      const float sm = ((a[0] + a[1]) + (a[2] + a[3])) + ((b[0] + b[1]) + (b[2] + b[3]));
      const bool bad = !(l > 5.4e-20f && l < 1.8e19f) || !(mx < 3.0e38f) || (sm != sm);
      if (__any(bad)) { if (lane == 0) *(volatile LDSP unsigned*)(smem + K2_EX + 8192) = 1u; }
      asm volatile("s_waitcnt lgkmcnt(0)\n\ts_barrier" ::: "memory");
      const unsigned redo = __builtin_amdgcn_readfirstlane(*(volatile LDSP unsigned*)(smem + K2_EX + 8192));
      if (redo) {
        u32x4 q1z = q1; if (h) { q1z[2] = 0u; q1z[3] = 0u; }
        const char* kb = ws + WS_KV + (size_t)head * 262144 + lane * 16;
        float mrow = -INFINITY;
        for (int t = 0; t < 64; ++t) {
          const f32x16 zero = {};
          f32x16 s = mfma32(*(const bf16x8*)(kb + t * 4096), asb(q0), zero);
          s = mfma32(*(const bf16x8*)(kb + t * 4096 + 1024), asb(q1z), s);
#pragma unroll
          for (int r = 0; r < 16; ++r) mrow = fmaxf(mrow, s[r]);
        }
        auto rr = __builtin_amdgcn_permlane32_swap(__float_as_uint(mrow), __float_as_uint(mrow), false, false);
        mrow = fmaxf(__uint_as_float(rr[0]), __uint_as_float(rr[1]));
        asm volatile("s_waitcnt vmcnt(0) lgkmcnt(0)\n\ts_barrier" ::: "memory");
#pragma unroll
        for (int st = 0; st < 4; ++st)
#pragma unroll
          for (int i = 0; i < 4; ++i) glds16(kvsrc + st * 32768 + i * 1024, lds0 + st * 32768 + (4 * w + i) * 1024);
        o = (f32x16){};
        attn_loop<true>(kvsrc, lds0, L, w, lane, asb(q0), asb(q1z), mrow, o);
        continue;
      }
    }
    *((u32x4*)(ws + WS_O + ((size_t)(qt * 16 + head) * 2 + par) * 1024) + lane) = (u32x4){pk2(a[0], a[1]), pk2(a[2], a[3]), pk2(b[0], b[1]), pk2(b[2], b[3])};
    break;
  }
}

DI void store_tile(u16* out, int row0, int col0, int lane, const f32x16& d) {
  const int r32 = lane & 31, h = lane >> 5;
  char* base = (char*)(out + (size_t)(row0 + r32) * 896 + col0) + h * 16;
#pragma unroll
  for (int k = 0; k < 2; ++k) {
    unsigned a0 = pk2(d[8 * k], d[8 * k + 1]), a1 = pk2(d[8 * k + 2], d[8 * k + 3]), b0 = pk2(d[8 * k + 4], d[8 * k + 5]), b1 = pk2(d[8 * k + 6], d[8 * k + 7]);
    auto s0 = __builtin_amdgcn_permlane32_swap(a0, b0, false, false);
    auto s1 = __builtin_amdgcn_permlane32_swap(a1, b1, false, false);
    *(u32x4*)(base + 32 * k) = (u32x4){s0[0], s1[0], s0[1], s1[1]};
  }
}

__global__ __launch_bounds__(256, 2) void k3(const char* __restrict__ ws, u16* __restrict__ out) {
  __shared__ __attribute__((aligned(1024))) char smem[57344];
  const LDSP char* L = (const LDSP char*)smem;
  const unsigned lds0 = (unsigned)(uintptr_t)(LDSP char*)smem;
  const int tid = threadIdx.x, lane = tid & 63, w = __builtin_amdgcn_readfirstlane(tid >> 6);
  const int xcd = blockIdx.x & 7, idx = blockIdx.x >> 3;
  const int sub = idx % 28, rbk = xcd * 4 + (sub & 3), nb = sub >> 2;
  if (idx < 28) {
    const int qt = rbk * 2 + (w >> 1), nt = nb * 2 + (w & 1);
    const bf16x8* op = (const bf16x8*)(ws + WS_O + (size_t)qt * 32768) + lane;
    const bf16x8* wp = (const bf16x8*)(ws + WS_WO + (size_t)nt * 32768) + lane;
    f32x16 acc = {};
#pragma unroll
    for (int half = 0; half < 2; ++half) {
      bf16x8 of[16], wf[16];
#pragma unroll
      for (int i = 0; i < 16; ++i) { of[i] = op[(half * 16 + i) * 64]; wf[i] = wp[(half * 16 + i) * 64]; }
#pragma unroll
      for (int i = 0; i < 16; ++i) acc = mfma32(wf[i], of[i], acc);
    }
    store_tile(out, qt * 32, 448 + nt * 32, lane, acc);
  } else {
#pragma unroll
    for (int i = 0; i < 14; ++i) {
      const int pl = (14 * w + i) * 8 + (lane >> 3);
      const char* src = ws + WS_Y1 + (size_t)pl * 4096 + rbk * 128 + (((lane & 7) ^ (((pl >> 1) & 1) << 2)) << 4);
      glds16(src, lds0 + (14 * w + i) * 1024);
    }
    const int rt = w >> 1, eo = w & 1;
    const bf16x8* gp = (const bf16x8*)(ws + WS_G + (size_t)(eo * 7 + nb) * 14336) + lane;
    bf16x8 gf[14];
#pragma unroll
    for (int i = 0; i < 14; ++i) gf[i] = gp[i * 64];
    const int h = lane >> 5, blk = (lane >> 4) & 1, q = (lane & 15) >> 2, p = lane & 3;
    const int cb = rt * 64 + 32 * blk + 8 * p;
    const unsigned ya = (eo * 224 + 8 * h + q) * 128 + ((((cb >> 4) ^ (((q >> 1) & 1) << 2)) & 7) << 4) + (cb & 8);
    WAITV_BAR(0);
    f32x16 acc = {};
#pragma unroll
    for (int i = 0; i < 14; ++i) {
      bf16x8 y = cat(ldtr(L + ya + i * 2048), ldtr(L + ya + i * 2048 + 512));
      acc = mfma32(gf[i], y, acc);
    }
    asm volatile("s_waitcnt lgkmcnt(0)\n\ts_barrier" ::: "memory");
    LDSP f32x4* ex = (LDSP f32x4*)(smem + w * 4096) + lane;
#pragma unroll
    for (int i = 0; i < 4; ++i) ex[i * 64] = (f32x4){acc[4 * i], acc[4 * i + 1], acc[4 * i + 2], acc[4 * i + 3]};
    asm volatile("s_waitcnt lgkmcnt(0)\n\ts_barrier" ::: "memory");
    const LDSP f32x4* ox = (const LDSP f32x4*)(smem + (w ^ 1) * 4096) + lane;
#pragma unroll
    for (int i = 0; i < 4; ++i) {
      f32x4 v = ox[i * 64];
#pragma unroll
      for (int r = 0; r < 4; ++r) acc[4 * i + r] = eo ? v[r] - acc[4 * i + r] : v[r] + acc[4 * i + r];
    }
    store_tile(out, rbk * 64 + rt * 32, nb * 32 + eo * 224, lane, acc);
  }
}

extern "C" void kernel_launch(void* const* d_in, const int* in_sizes, int n_in, void* d_out, int out_size, void* d_ws, size_t ws_size,
                              hipStream_t stream) {
  const u16* ax = (const u16*)d_in[0]; const u16* wr = (const u16*)d_in[1]; const u16* wi = (const u16*)d_in[2];
  const u16* bx = (const u16*)d_in[3]; const u16* wqkv = (const u16*)d_in[4]; const u16* wo = (const u16*)d_in[5];
  k1<<<256, 256, 0, stream>>>(ax, wr, wi, bx, wqkv, wo, (char*)d_ws);
  k2<<<256, 512, 0, stream>>>((char*)d_ws);
  k3<<<448, 256, 0, stream>>>((const char*)d_ws, (u16*)d_out);
}
```

```cpp
#include <hip/hip_runtime.h>
#include <stdint.h>
#include <type_traits>

typedef unsigned short u16;
typedef short bf16x8 __attribute__((ext_vector_type(8)));
typedef short s16x4 __attribute__((ext_vector_type(4)));
typedef float f32x16 __attribute__((ext_vector_type(16)));
typedef float f32x4 __attribute__((ext_vector_type(4)));
typedef float f32x2 __attribute__((ext_vector_type(2)));
typedef unsigned u32x4 __attribute__((ext_vector_type(4)));
typedef __bf16 bf16x2_t __attribute__((ext_vector_type(2)));
#define LDSP __attribute__((address_space(3)))
#define GAS __attribute__((address_space(1)))
#define DI __device__ __forceinline__

constexpr int NF = 225;
constexpr unsigned WS_KV = 0;
constexpr unsigned WS_X1 = 4u << 20;
constexpr unsigned WS_WT = 6u << 20;
constexpr unsigned WS_Y1 = 8u << 20;
constexpr unsigned WS_O = 10u << 20;
constexpr unsigned WS_FL = 12u << 20;
constexpr unsigned WS_USED = WS_FL + 16384;
constexpr unsigned FL_KV = WS_FL + 1024, FL_A = WS_FL + 4096, FL_Y = WS_FL + 6144, FL_O = WS_FL + 8192;
constexpr int L_WQ = 49152, L_XA = 77824, L_EX = 131072, L_T2 = 139264, L_QX = 147456, L_WR = 155648, L_CNT = L_WR + 7200;
constexpr int L_WO = 0, L_Y1 = 57344, L_OS = 57344;
constexpr int LDS_BYTES = 163840;
constexpr int SPIN_CAP = 1 << 18;

#define STAMP(I)
#define STAMPL(I)

DI unsigned pk2(float lo, float hi) { bf16x2_t v = __builtin_convertvector((f32x2){lo, hi}, bf16x2_t); return __builtin_bit_cast(unsigned, v); }
DI float bflo(unsigned u) { return __uint_as_float(u << 16); }
DI float bfhi(unsigned u) { return __uint_as_float(u & 0xffff0000u); }
DI f32x16 mfma32(bf16x8 a, bf16x8 b, f32x16 c) { return __builtin_amdgcn_mfma_f32_32x32x16_bf16(a, b, c, 0, 0, 0); }
DI f32x4 mfma16(bf16x8 a, bf16x8 b, f32x4 c) { return __builtin_amdgcn_mfma_f32_16x16x32_bf16(a, b, c, 0, 0, 0); }
DI bf16x8 asb(u32x4 v) { return __builtin_bit_cast(bf16x8, v); }
DI u32x4 asu(bf16x8 v) { return __builtin_bit_cast(u32x4, v); }
DI int phi(int rt, int rho) { return 8 * (rho >> 2) + 4 * rt + (rho & 3); }
DI float cosr(float rev) { return __builtin_amdgcn_cosf(rev); }
DI bf16x8 ldsr(const LDSP char* p) { return *(const LDSP bf16x8*)p; }
DI s16x4 ldtr(const LDSP char* p) { return __builtin_amdgcn_ds_read_tr16_b64_v4i16((LDSP s16x4*)p); }
DI bf16x8 cat(s16x4 a, s16x4 b) { return __builtin_shufflevector(a, b, 0, 1, 2, 3, 4, 5, 6, 7); }
DI float swapadd(float v) {
  auto r = __builtin_amdgcn_permlane32_swap(__float_as_uint(v), __float_as_uint(v), false, false);
  return __uint_as_float(r[0]) + __uint_as_float(r[1]);
}
DI u32x4 pack8(const f32x16& x, int o) { return (u32x4){pk2(x[o], x[o + 1]), pk2(x[o + 2], x[o + 3]), pk2(x[o + 4], x[o + 5]), pk2(x[o + 6], x[o + 7])}; }
DI u32x4 pack44(f32x4 a, f32x4 b) { return (u32x4){pk2(a[0], a[1]), pk2(a[2], a[3]), pk2(b[0], b[1]), pk2(b[2], b[3])}; }
DI void dma1(const void* base, unsigned voff, unsigned lds) {
  unsigned sv;
  asm volatile("s_mov_b32 %0, m0\n\ts_mov_b32 m0, %3\n\ts_nop 0\n\tglobal_load_lds_dwordx4 %1, %2\n\ts_mov_b32 m0, %0"
               : "=&s"(sv) : "v"(voff), "s"(base), "s"(lds) : "memory");
}
DI void dma1c(const void* base, unsigned voff, unsigned lds) {
  unsigned sv;
  asm volatile("s_mov_b32 %0, m0\n\ts_mov_b32 m0, %3\n\ts_nop 0\n\tglobal_load_lds_dwordx4 %1, %2 sc1\n\ts_mov_b32 m0, %0"
               : "=&s"(sv) : "v"(voff), "s"(base), "s"(lds) : "memory");
}
DI void dma4c(const void* base, unsigned voff, unsigned lds) {
  unsigned sv;
  asm volatile("s_mov_b32 %0, m0\n\ts_mov_b32 m0, %3\n\ts_nop 0\n\tglobal_load_lds_dwordx4 %1, %2 sc1\n\tglobal_load_lds_dwordx4 %1, %2 offset:1024 sc1\n\t"
               "global_load_lds_dwordx4 %1, %2 offset:2048 sc1\n\tglobal_load_lds_dwordx4 %1, %2 offset:3072 sc1\n\ts_mov_b32 m0, %0"
               : "=&s"(sv) : "v"(voff), "s"(base), "s"(lds) : "memory");
}
#define WAITV(N) asm volatile("s_waitcnt vmcnt(" #N ")" ::: "memory")
#define BAR() asm volatile("s_waitcnt lgkmcnt(0)\n\ts_barrier" ::: "memory")
#define BARX() asm volatile("s_barrier" ::: "memory")
#define BARQ() __builtin_amdgcn_s_sleep(1)
#define SB() __builtin_amdgcn_sched_barrier(0)
#define RS __amdgpu_buffer_rsrc_t
DI u32x4 ldc(RS r, unsigned off) { return __builtin_amdgcn_raw_buffer_load_b128(r, off, 0, 16); }
DI unsigned ldc1(RS r, unsigned off) { return __builtin_amdgcn_raw_buffer_load_b32(r, off, 0, 16); }
DI void stc(RS r, unsigned off, u32x4 v) { __builtin_amdgcn_raw_buffer_store_b128(v, r, off, 0, 16); }
DI void stc1(RS r, unsigned off, unsigned v) { __builtin_amdgcn_raw_buffer_store_b32(v, r, off, 0, 16); }
DI unsigned lds_arrive(LDSP unsigned* c) {
  unsigned old = 0;
  if ((threadIdx.x & 63) == 0) old = __hip_atomic_fetch_add(c, 1u, __ATOMIC_RELAXED, __HIP_MEMORY_SCOPE_WORKGROUP);
  return __builtin_amdgcn_readfirstlane(old);
}
DI void lds_wait(LDSP unsigned* c, unsigned n) {
#pragma unroll 1
  while (__builtin_amdgcn_readfirstlane(__hip_atomic_load(c, __ATOMIC_RELAXED, __HIP_MEMORY_SCOPE_WORKGROUP)) < n) __builtin_amdgcn_s_sleep(1);
  asm volatile("" ::: "memory");
}
DI void store_half(u16* out, int row0, int col0, int lane, f32x4 a, f32x4 b) {
  unsigned a0 = pk2(a[0], a[1]), a1 = pk2(a[2], a[3]), b0 = pk2(b[0], b[1]), b1 = pk2(b[2], b[3]);
  auto s0 = __builtin_amdgcn_permlane32_swap(a0, b0, false, false);
  auto s1 = __builtin_amdgcn_permlane32_swap(a1, b1, false, false);
  *(u32x4*)((char*)(out + (size_t)(row0 + (lane & 31)) * 896 + col0) + (lane >> 5) * 16) = (u32x4){s0[0], s1[0], s0[1], s1[1]};
}
#define PIN(x) asm volatile("" : "+v"(x))
#define EX2(v) __builtin_amdgcn_exp2f(v)

__global__ __launch_bounds__(512, 2) void fused(const u16* __restrict__ ax, const u16* __restrict__ wr, const u16* __restrict__ wi,
                                                const u16* __restrict__ bx, const u16* __restrict__ wqkv, const u16* __restrict__ wo,
                                                u16* __restrict__ out, char* __restrict__ ws) {
  __shared__ __attribute__((aligned(1024))) char smem[LDS_BYTES];
  LDSP char* L = (LDSP char*)smem;
  const unsigned lds0 = (unsigned)(uintptr_t)L;
  const int tid = threadIdx.x, lane = tid & 63, w = __builtin_amdgcn_readfirstlane(tid >> 6);
  const int r32 = lane & 31, h = lane >> 5, l16 = lane & 15, g = lane >> 4;
  const int bid = blockIdx.x, xcd = bid & 7, bi = bid >> 3, head = 2 * xcd + (bi & 1), jb = bi >> 1;
  const int w4 = w & 3;
  const bool lo = w < 4;
  const RS rs = __builtin_amdgcn_make_buffer_rsrc(ws, 0, WS_USED, 0x00020000);
  STAMP(0);
  const unsigned xv0 = (unsigned)((16 * w + (lane >> 3)) * 896 + (((lane & 7) ^ (lane >> 4)) << 4));
  const unsigned xv1 = (unsigned)((16 * w + 8 + (lane >> 3)) * 896 + (((lane & 7) ^ (4 + (lane >> 4))) << 4));
  const int wch = (lane & 7) ^ (((lane >> 4) & 1) << 2);
  const unsigned wv = (unsigned)((8 * w + (lane >> 3)) * 2688 + ((896 * (1 + (wch >> 2)) + 56 * head) & ~15) + (wch & 3) * 16);
  const char* xb = (const char*)bx + (size_t)jb * (128 * 896);
  auto issue = [&](int c, int slot) {
    const unsigned d = lds0 + slot * 24576;
    dma1((const char*)wqkv + c * (64 * 2688), wv, d + 16384 + w * 1024);
    dma1(xb + c * 128, xv0, d + w * 2048);
    dma1(xb + c * 128, xv1, d + w * 2048 + 1024);
  };
  issue(0, 0); BARX(); issue(1, 1); BARX(); issue(2, 2);
  if (tid < 8) *(LDSP unsigned*)(L + L_CNT + tid * 4) = 0u;
  unsigned tag;
  STAMP(31);

  const int kd = bid;
  const bool dofft = kd < NF;
  const bool dotail = bi < 28;
  const int rbk = xcd * 4 + (bi & 3), nb = bi >> 2;
  const int rtA = xcd * 8 + (bi & 7), kg = bi >> 3;
  const int dlt = (56 * head) & 8;
  const int blk = (lane >> 4) & 1, q = l16 >> 2, p = lane & 3;
  const unsigned xsw = (unsigned)((r32 * 128) + (((h ^ (r32 >> 1)) & 7) << 4));
  const char* kvb = ws + WS_KV + (size_t)head * 262144;
  const int par = w >> 2;
  const f32x16 zero = {};
  LDSP unsigned* cnt = (LDSP unsigned*)(L + L_CNT);

  bf16x8 gf[7];
  const float gdf = (float)(nb * 32 + r32) * (1.0f / 448.0f);
  const float ga0 = gdf * (float)(8 * h + ((w >> 1) & 1) + 112 * (w & 1)), ga1 = ga0 + 0.25f;
  auto gen_g = [&](int i) {
    float v[8];
#pragma unroll
    for (int j = 0; j < 8; ++j) v[j] = cosr(__builtin_fmaf(gdf, (float)(16 * i + (j & ~1)), (j & 1) ? ga1 : ga0));
    if (i == 0 && (w & 3) == 0 && h == 0) { v[0] = 0.5f; v[1] = (r32 & 1) ? -0.5f : 0.5f; }
    gf[i] = asb((u32x4){pk2(v[0], v[1]), pk2(v[2], v[3]), pk2(v[4], v[5]), pk2(v[6], v[7])});
    PIN(gf[i]);
  };
  auto gen_t = [&](float af, float a0, int b, bf16x8& f0, bf16x8& f1) {
    float c[4], sn[4];
#pragma unroll
    for (int r = 0; r < 4; ++r) { const float an = __builtin_fmaf(af, (float)(b + r), a0); c[r] = cosr(an); sn[r] = __builtin_amdgcn_sinf(an); }
    const unsigned c0 = pk2(c[0], c[1]), c1 = pk2(c[2], c[3]);
    f0 = asb((u32x4){c0, c1, pk2(sn[0], sn[1]), pk2(sn[2], sn[3])});
    f1 = asb((u32x4){pk2(-sn[0], -sn[1]), pk2(-sn[2], -sn[3]), c0, c1});
    PIN(f0); PIN(f1);
  };
  u32x4 q0, q1;
  f32x16 o = {};
#define E4(d, S, i) d[0] = EX2(S[i]); d[1] = EX2(S[i + 1]); d[2] = EX2(S[i + 2]); d[3] = EX2(S[i + 3]);
#define C2(pp, i, e) pp[i] = pk2(e[0], e[1]); pp[i + 1] = pk2(e[2], e[3]);
#define TILE(SC, SN, KP, VBP, VAP) {                                                        \
    SN = mfma32(ka, qa, zero); ka = ldsr(KP); SB(); C2(p1, 2, eb) SB(); E4(ea, SC, 0) SB();     \
    o = mfma32(vb, asb(p1), o); vb = ldsr(VBP); SB(); C2(p0, 0, ea) SB(); E4(eb, SC, 4) SB();   \
    SN = mfma32(kb, qb, SN); kb = ldsr((KP) + 1024); SB(); C2(p0, 2, eb) SB(); E4(ea, SC, 8) SB(); \
    o = mfma32(va, asb(p0), o); va = ldsr(VAP); SB(); C2(p1, 0, ea) SB(); E4(eb, SC, 12) SB(); }
  auto flags_ok = [&](int ldsoff, int n) {
    const u32x4 f = *(const volatile LDSP u32x4*)(L + ldsoff + lane * 16);
    return __all((4 * lane >= n || f[0] == tag) && (4 * lane + 1 >= n || f[1] == tag) && (4 * lane + 2 >= n || f[2] == tag) && (4 * lane + 3 >= n || f[3] == tag));
  };
  auto flags_wait = [&](unsigned fo, int ldsoff, int n) {
#pragma unroll 1
    for (int spin = 0; spin < SPIN_CAP && !flags_ok(ldsoff, n); ++spin) {
      __builtin_amdgcn_s_sleep(2);
      dma1c(ws + fo, lane * 16, lds0 + ldsoff);
      WAITV(0);
    }
    asm volatile("" ::: "memory");
  };
  auto attention = [&](auto hi_tag, const bf16x8* t1, const f32x4* tw) {
    constexpr bool HI = decltype(hi_tag)::value;
    const bf16x8 qa = asb(q0), qb = asb(q1);
    const unsigned toff = par * 8192 + lane * 16;
    {
      const LDSP char* c = L + toff;
      f32x16 s0 = mfma32(ldsr(c), qa, zero), s1 = mfma32(ldsr(c + 4096), qa, zero);
      s0 = mfma32(ldsr(c + 1024), qb, s0); s1 = mfma32(ldsr(c + 5120), qb, s1);
#pragma unroll
      for (int r = 0; r < 16; ++r) s0[r] = EX2(s0[r]);
      o = mfma32(ldsr(c + 2048), asb(pack8(s0, 0)), o); o = mfma32(ldsr(c + 3072), asb(pack8(s0, 8)), o);
#pragma unroll
      for (int r = 0; r < 16; ++r) s1[r] = EX2(s1[r]);
      o = mfma32(ldsr(c + 6144), asb(pack8(s1, 0)), o); o = mfma32(ldsr(c + 7168), asb(pack8(s1, 8)), o);
    }
    STAMP(5);
    f32x16 sA, sB;
    f32x4 ea, eb = {};
    u32x4 p0, p1 = {};
    bf16x8 ka, kb, va, vb = {};
    if (!HI) WAITV(4);
    BAR();
    dma4c(kvb + ((jb + 6 + par) & 15) * 16384 + w4 * 4096, lane * 16, lds0 + (6 + par) * 16384 + w4 * 4096);
    {
      const LDSP char* c = L + 16384 + toff;
      sA = mfma32(ldsr(c), qa, zero); sA = mfma32(ldsr(c + 1024), qb, sA);
      ka = ldsr(c + 4096); kb = ldsr(c + 5120); va = ldsr(c + 2048);
    }
    auto step = [&](auto ic) {
      constexpr int i = decltype(ic)::value;
      const LDSP char* c = L + (i & 7) * 16384 + toff;
      const LDSP char* n = L + ((i + 1) & 7) * 16384 + toff;
      SB();
      TILE(sA, sB, n, c + 3072, c + 4096 + 2048)
      TILE(sB, sA, n + 4096, c + 4096 + 3072, n + 2048)
      PIN(eb); PIN(p1);
      constexpr int nx = i + 1;
      if (nx & 1) {
        if (nx < 13) { if (!(HI && nx == 7 && dofft)) WAITV(4); } else WAITV(0);
        BAR();
        if (HI && nx == 9 && dofft) {
          const f32x4 z4 = {};
          f32x4 orr[2], oi[2];
#pragma unroll
          for (int rt = 0; rt < 2; ++rt) {
            orr[rt] = z4; oi[rt] = z4;
#pragma unroll
            for (int wp = 0; wp < 4; ++wp) {
              const bf16x8 u = ldsr(L + L_EX + (wp * 2 + rt) * 1024 + lane * 16);
              orr[rt] = mfma16(u, t1[1 + 2 * wp], orr[rt]); oi[rt] = mfma16(u, t1[2 * wp], oi[rt]);
            }
          }
          const int pre = (kd & 1) ? 223 + kd : (kd == 224 ? 1 : kd);
          const unsigned yo = WS_Y1 + pre * 4096 + (16 * w4 + l16) * 64 + g * 16;
          stc(rs, yo, pack44(orr[0], orr[1]));
          if (kd != 0 && kd != 224) stc(rs, yo + 4096, pack44(oi[0], oi[1]));
        }
        if (nx <= 9) dma4c(kvb + ((jb + nx + 5 + par) & 15) * 16384 + w4 * 4096, lane * 16, lds0 + ((nx + 5 + par) & 7) * 16384 + w4 * 4096);
        if (HI && nx == 5 && dofft) dma1c(ws + FL_A, lane * 16, lds0 + L_QX + w4 * 2048);
        if (HI && nx == 11 && dofft && tid == 256) stc1(rs, FL_Y + kd * 4, tag);
        if (nx == 13 && dotail) {
          const int row = 8 * w + (lane >> 3);
          const unsigned ov = (unsigned)(row * 896 + nb * 128 + (((lane & 7) ^ (((row >> 1) & 1) << 2)) << 4));
#pragma unroll
          for (int t = 0; t < 7; ++t) dma1((const char*)wo + t * (64 * 896), ov, lds0 + L_WO + t * 8192 + w * 1024);
        }
        if (nx == 15 && dotail) {
          flags_wait(FL_Y, L_QX + w * 1024, NF);
          const int pl = 8 * w + (lane >> 3);
          const unsigned yv = (unsigned)(pl * 4096 + rbk * 128 + (((lane & 7) ^ (((pl >> 1) & 1) << 2)) << 4));
#pragma unroll
          for (int t = 0; t < 7; ++t) dma1c(ws + WS_Y1 + t * (64 * 4096), yv, lds0 + L_Y1 + t * 8192 + w * 1024);
        }
      } else {
        if (nx == 2) {
          if (HI) { WAITV(12); if (lds_arrive(cnt + 4) == 3 && lane == 0) stc1(rs, FL_A + bid * 4, tag); }
          else WAITV(4);
          BAR();
        }
        if (nx == 14 && dotail) dma1c(ws + FL_Y, lane * 16, lds0 + L_QX + w * 1024);
        if (HI && nx == 6 && dofft) {
          WAITV(0);
          flags_wait(FL_A, L_QX + w4 * 2048, 256);
          dma1c(ws + WS_X1 + kd * 8192 + w4 * 2048, lane * 16, lds0 + L_QX + w4 * 2048);
          dma1c(ws + WS_X1 + kd * 8192 + w4 * 2048 + 1024, lane * 16, lds0 + L_QX + w4 * 2048 + 1024);
          dma1c(ws + WS_WT + kd * 8192 + w4 * 64, (lane >> 2) * 256 + (lane & 3) * 16, lds0 + L_EX + w4 * 2048);
          dma1c(ws + WS_WT + kd * 8192 + 4096 + w4 * 64, (lane >> 2) * 256 + (lane & 3) * 16, lds0 + L_EX + w4 * 2048 + 1024);
        }
        if (HI && nx == 8 && dofft) {
          WAITV(4);
          lds_arrive(cnt + 5);
          lds_wait(cnt + 5, 4);
          const f32x4 z4 = {};
          bf16x8 zf[2], yf[2];
#pragma unroll
          for (int rt = 0; rt < 2; ++rt) {
            f32x4 zr = z4, zi = z4;
            const LDSP char* xp = L + L_QX + phi(rt, l16) * 256 + g * 16;
#pragma unroll
            for (int wp = 0; wp < 4; ++wp) { const bf16x8 x = ldsr(xp + wp * 64); zr = mfma16(x, t1[2 * wp], zr); zi = mfma16(x, t1[1 + 2 * wp], zi); }
            const f32x4 c = tw[2 * rt], sn = tw[2 * rt + 1];
            zf[rt] = asb(pack44(zr * c + zi * sn, zi * c - zr * sn));
          }
          const LDSP char* t2 = L + L_T2 + lane * 16;
          const LDSP unsigned* wt = (const LDSP unsigned*)(L + L_EX + w4 * 2048 + g * 512) + l16;
          bf16x8 t2f[8];
#pragma unroll
          for (int f = 0; f < 8; ++f) t2f[f] = ldsr(t2 + f * 1024);
#pragma unroll
          for (int ta = 0; ta < 2; ++ta) {
            f32x4 yr = mfma16(t2f[4 * ta], zf[0], z4), yi = mfma16(t2f[4 * ta + 1], zf[0], z4);
            yr = mfma16(t2f[4 * ta + 2], zf[1], yr); yi = mfma16(t2f[4 * ta + 3], zf[1], yi);
            const unsigned w0 = wt[64 * ta], w1 = wt[64 * ta + 16], w2 = wt[64 * ta + 32], w3 = wt[64 * ta + 48];
            const f32x4 c = {bflo(w0), bflo(w1), bflo(w2), bflo(w3)}, sn = {bfhi(w0), bfhi(w1), bfhi(w2), bfhi(w3)};
            yf[ta] = asb(pack44(yr * sn + yi * c, yr * c - yi * sn));
          }
#pragma unroll
          for (int rt = 0; rt < 2; ++rt) {
            f32x4 ur = mfma16(yf[0], t2f[4 * rt + 1], z4), ui = mfma16(yf[0], t2f[4 * rt], z4);
            ur = mfma16(yf[1], t2f[4 * rt + 3], ur); ui = mfma16(yf[1], t2f[4 * rt + 2], ui);
            const f32x4 c = tw[4 + 2 * rt], sn = tw[5 + 2 * rt];
            *((LDSP u32x4*)(L + L_EX + (w4 * 2 + rt) * 1024) + lane) = pack44(ui * c + ur * sn, ur * c - ui * sn);
          }
        }
      }
      STAMPL(14 + nx);
    };
#define IC(n) std::integral_constant<int, n>{}
    step(IC(1)); step(IC(2)); step(IC(3)); step(IC(4)); step(IC(5)); step(IC(6)); step(IC(7)); step(IC(8));
    step(IC(9)); step(IC(10)); step(IC(11)); step(IC(12)); step(IC(13)); step(IC(14)); step(IC(15));
    C2(p1, 2, eb)
    o = mfma32(vb, asb(p1), o);
  };

  if (lo) {
    bf16x8 xf[28];
    f32x16 acck = {}, accv = {};
    const int bk = dlt + 32 * blk + 8 * p, bv = bk + 64;
    const unsigned ka0 = 16384 + (8 * h + q) * 128 + ((((bk >> 4) ^ ((q >> 1) << 2)) & 7) << 4) + (bk & 8);
    const unsigned va0 = 16384 + (8 * h + q) * 128 + ((((bv >> 4) ^ ((q >> 1) << 2)) & 7) << 4) + (bv & 8);
#pragma unroll
    for (int c = 0; c < 7; ++c) {
      if (c <= 3) WAITV(6); else if (c == 4) WAITV(6); else if (c == 5) WAITV(3); else WAITV(0);
      BAR();
      if (c == 0) STAMP(11);
      if (c == 5) STAMP(13);
      if (c < 4) issue(c + 3, (c + 3) % 6);
      const LDSP char* sb = L + (c % 6) * 24576;
#pragma unroll
      for (int kk = 0; kk < 4; ++kk) {
        const bf16x8 x = ldsr(sb + w * 4096 + (xsw ^ (kk << 5)));
        const bf16x8 fk = cat(ldtr(sb + ka0 + kk * 2048), ldtr(sb + ka0 + kk * 2048 + 512));
        const bf16x8 fv = cat(ldtr(sb + va0 + kk * 2048), ldtr(sb + va0 + kk * 2048 + 512));
        acck = mfma32(fk, x, acck);
        accv = mfma32(x, fv, accv);
        xf[4 * c + kk] = x;
      }
      gen_g(c);
      SB();
    }
    STAMP(1);
    {
      u32x4 k0 = pack8(acck, 0), k1 = pack8(acck, 8), v0 = pack8(accv, 0), v1 = pack8(accv, 8);
      if (h) { k1[2] = 0x00003f80u; k1[3] = 0u; }
      if (r32 >= 28) { const unsigned f = r32 == 28 ? 0x3f803f80u : 0u; v0 = (u32x4){f, f, f, f}; v1 = v0; }
      const unsigned ko = WS_KV + (unsigned)(head * 64 + jb * 4 + w) * 4096 + lane * 16;
      stc(rs, ko, k0); stc(rs, ko + 1024, k1); stc(rs, ko + 2048, v0); stc(rs, ko + 3072, v1);
      LDSP u32x4* own = (LDSP u32x4*)(L + w * 4096) + lane;
      own[0] = k0; own[64] = k1; own[128] = v0; own[192] = v1;
    }
    tag = __builtin_amdgcn_readfirstlane(*(const volatile LDSP unsigned*)(L + L_CNT + 32)) + 1u;
    WAITV(0);
    if (lds_arrive(cnt) == 3 && lane == 0) stc1(rs, FL_KV + head * 128 + jb * 4, tag);
    STAMP(2);
    BAR();
    f32x16 accq = {};
    const LDSP char* qa = L + L_WQ + (8 * h + q) * 64 + dlt + 32 * blk + 8 * p;
#pragma unroll
    for (int ks = 0; ks < 28; ++ks) accq = mfma32(cat(ldtr(qa + ks * 1024), ldtr(qa + ks * 1024 + 256)), xf[ks], accq);
    accq *= 0.18898223650461362f * 1.4426950408889634f;
    float sd = 0.f, s2 = 0.f;
#pragma unroll
    for (int r = 0; r < 12; ++r) sd += accq[r] * acck[r];
#pragma unroll
    for (int r = 12; r < 16; ++r) s2 += accq[r] * acck[r];
    sd = swapadd(sd + (h ? 0.f : s2));
    q0 = pack8(accq, 0); q1 = pack8(accq, 8);
    if (h) { q1[2] = pk2(-sd, 0.f); q1[3] = 0u; }
    LDSP u32x4* qx = (LDSP u32x4*)(L + L_QX + w * 2048) + lane;
    qx[0] = q0; qx[64] = q1;
    STAMP(12);
    if (w == 0) {
#pragma unroll 1
      for (int spin = 0; spin < SPIN_CAP; ++spin) {
        const unsigned v = ldc1(rs, FL_KV + head * 128 + l16 * 4);
        if (__all(v == tag)) break;
        __builtin_amdgcn_s_sleep(2);
      }
      asm volatile("" ::: "memory");
      lds_arrive(cnt + 6);
    } else {
      lds_wait(cnt + 6, 1);
    }
    STAMP(3);
    dma4c(kvb + ((jb + 1) & 15) * 16384 + w * 4096, lane * 16, lds0 + 16384 + w * 4096);
    dma4c(kvb + ((jb + 2) & 15) * 16384 + w * 4096, lane * 16, lds0 + 32768 + w * 4096);
    BAR();
    STAMP(4);
    dma4c(kvb + ((jb + 3) & 15) * 16384 + w * 4096, lane * 16, lds0 + 49152 + w * 4096);
    attention(std::false_type{}, nullptr, nullptr);
  } else {
    bf16x8 tab[14], t1[8];
    f32x4 tw[8];
    const float kf = (float)(32 * kg + r32) * (1.0f / 448.0f);
    const float fa0 = __builtin_fmaf((float)(8 * h + (w4 & 1)), kf, (w4 & 2) ? -0.25f : 0.f);
    const float taf = (float)(16 * w4 + l16) * (1.0f / 64.0f), ta0 = taf * (float)(4 * g);
#pragma unroll
    for (int c = 0; c < 7; ++c) {
      if (c <= 4) WAITV(6); else if (c == 5) WAITV(10); else WAITV(16);
      BAR();
      if (c < 4) issue(c + 3, (c + 3) % 6);
      if (c == 3 && tid == 448) dma1c(ws + WS_FL, 0, lds0 + L_CNT + 32);
      if (c == 4) {
        const unsigned qv = (unsigned)((lane >> 2) * 2688 + ((56 * head) & ~15) + (lane & 3) * 16);
#pragma unroll
        for (int i = 0; i < 7; ++i) dma1((const char*)wqkv + (w4 * 7 + i) * (16 * 2688), qv, lds0 + L_WQ + (w4 * 7 + i) * 1024);
      }
      if (c == 5) {
        const int rr = 8 * w4 + (lane >> 3);
        const unsigned av = (unsigned)((32 * (32 * (rtA & 1) + rr) + (rtA >> 1)) * 896 + (((lane & 7) ^ ((rr >> 1) & 7)) << 4));
#pragma unroll
        for (int i = 0; i < 7; ++i) dma1((const char*)ax + i * 128, av, lds0 + L_XA + i * 4096 + w4 * 1024);
        const unsigned fo = (unsigned)(w4 * 1024 + lane * 16);
        if (fo < 3600u) {
          dma1((const char*)wr + bid * 3600, fo, lds0 + L_WR + w4 * 1024);
          dma1((const char*)wi + bid * 3600, fo, lds0 + L_WR + 3600 + w4 * 1024);
        }
      }
      gen_g(c);
#pragma unroll
      for (int u = 2 * c; u < 2 * c + 2; ++u) {
        float v[8];
#pragma unroll
        for (int j = 0; j < 8; ++j) v[j] = cosr(__builtin_fmaf(kf, (float)(32 * u + 2 * (j & 3) + 16 * (j >> 2)), fa0));
        tab[u] = asb((u32x4){pk2(v[0], v[1]), pk2(v[2], v[3]), pk2(v[4], v[5]), pk2(v[6], v[7])});
        PIN(tab[u]);
      }
      SB();
    }
    STAMP(1);
#pragma unroll
    for (int c = 0; c < 4; ++c) gen_t(taf, ta0, 16 * c, t1[2 * c], t1[2 * c + 1]);
    WAITV(9);
    BAR();
#pragma unroll
    for (int dir = 0; dir < 2; ++dir)
#pragma unroll
      for (int rt = 0; rt < 2; ++rt) {
#pragma unroll
        for (int r = 0; r < 4; ++r) {
          const int n2 = dir ? phi(rt, l16) : phi(rt, 4 * g + r), k1 = dir ? 16 * w4 + 4 * g + r : 16 * w4 + l16;
          const float an = (float)(n2 * k1) * (1.0f / 2048.0f);
          tw[dir * 4 + rt * 2][r] = cosr(an); tw[dir * 4 + rt * 2 + 1][r] = __builtin_amdgcn_sinf(an);
        }
        PIN(tw[dir * 4 + rt * 2]); PIN(tw[dir * 4 + rt * 2 + 1]);
      }
    tag = __builtin_amdgcn_readfirstlane(*(const volatile LDSP unsigned*)(L + L_CNT + 32)) + 1u;
    WAITV(0);
    lds_arrive(cnt + 1);
    lds_wait(cnt + 1, 4);
    STAMP(2);
#pragma unroll
    for (int i = 0; i < 2; ++i) {
      const int it = (w4 * 2 + i) * 64 + lane;
      if (it < 450) {
        const int k = it >> 1, hf = it & 1;
        const LDSP u16* a = (const LDSP u16*)(L + L_WR + hf * 1800) + k;
        const LDSP u16* b = (const LDSP u16*)(L + L_WR + 3600 + hf * 1800) + k;
        stc(rs, WS_WT + k * 8192 + bid * 32 + hf * 16,
            (u32x4){a[0] | ((unsigned)b[0] << 16), a[225] | ((unsigned)b[225] << 16), a[450] | ((unsigned)b[450] << 16), a[675] | ((unsigned)b[675] << 16)});
      }
    }
    f32x16 acc = {};
    const unsigned sel = (w4 & 1) ? 0x07060302u : 0x05040100u;
#pragma unroll
    for (int u = 0; u < 14; ++u) {
      const LDSP char* xa = L + L_XA + (u >> 1) * 4096;
      const u32x4 a = asu(ldsr(xa + (xsw ^ ((u & 1) << 6)))), b = asu(ldsr(xa + (xsw ^ ((u & 1) << 6) ^ 32)));
      const u32x4 e = {__builtin_amdgcn_perm(a[1], a[0], sel), __builtin_amdgcn_perm(a[3], a[2], sel), __builtin_amdgcn_perm(b[1], b[0], sel), __builtin_amdgcn_perm(b[3], b[2], sel)};
      acc = mfma32(asb(e), tab[u], acc);
    }
    LDSP f32x4* ex = (LDSP f32x4*)(L + L_EX + w4 * 4096) + lane;
#pragma unroll
    for (int i = 0; i < 4; ++i) ex[i * 64] = (f32x4){acc[4 * i], acc[4 * i + 1], acc[4 * i + 2], acc[4 * i + 3]};
    asm volatile("s_waitcnt lgkmcnt(0)" ::: "memory");
    lds_arrive(cnt + 3);
    lds_wait(cnt + 3, 4);
    {
      const int kl = w4 * 8 + (lane >> 3), pc = lane & 7, k = 32 * kg + kl;
      const LDSP f32x4* e = (const LDSP f32x4*)(L + L_EX + (pc >> 1) * 1024 + (kl + 32 * (pc & 1)) * 16);
      const f32x4 ce = e[0], co = e[256], se = e[512], so = e[768];
      const unsigned xo = WS_X1 + (rtA >> 1) * 256 + (rtA & 1) * 128 + pc * 16;
      if (k <= 112) stc(rs, xo + k * 8192, pack44(ce + co, -se - so));
      if (k < 112) stc(rs, xo + (224 - k) * 8192, pack44(ce - co, se - so));
    }
    {
      const u32x4 sen = {0xffffffffu, 0xffffffffu, 0xffffffffu, 0xffffffffu};
      const unsigned oo = WS_O + (unsigned)((jb * 4 + w4) * 16 + head) * 2048 + lane * 16;
      stc(rs, oo, sen); stc(rs, oo + 1024, sen);
    }
    bf16x8 t2a, t2b;
    { const float af = (float)phi(w4 >> 1, l16) * (1.0f / 32.0f); gen_t(af, af * (float)(8 * g), 4 * (w4 & 1), t2a, t2b); }
    STAMP(3);
    BAR();
    STAMP(4);
    __builtin_amdgcn_s_setprio(1);
    dma4c(kvb + ((jb + 4) & 15) * 16384 + w4 * 4096, lane * 16, lds0 + 65536 + w4 * 4096);
    dma4c(kvb + ((jb + 5) & 15) * 16384 + w4 * 4096, lane * 16, lds0 + 81920 + w4 * 4096);
    const LDSP u32x4* qx = (const LDSP u32x4*)(L + L_QX + w4 * 2048) + lane;
    q0 = qx[0]; q1 = qx[64];
    *((LDSP bf16x8*)(L + L_T2 + w4 * 2048) + lane) = t2a;
    *((LDSP bf16x8*)(L + L_T2 + w4 * 2048 + 1024) + lane) = t2b;
    attention(std::true_type{}, t1, tw);
  }
#undef TILE
  const bf16x8 qa = asb(q0);
  u32x4 fx[8];
  STAMP(6);
  __builtin_amdgcn_s_setprio(0);
  for (int pass = 0;; ++pass) {
    LDSP f32x4* ex = (LDSP f32x4*)(L + L_EX + w4 * 4096) + lane;
    LDSP float* el = (LDSP float*)(L + L_WR + w4 * 256) + lane;
    const float lme = o[12];
    if (par == 0) { ex[0] = (f32x4){o[8], o[9], o[10], o[11]}; ex[64] = (f32x4){o[12], o[13], o[14], o[15]}; }
    else { ex[128] = (f32x4){o[0], o[1], o[2], o[3]}; ex[192] = (f32x4){o[4], o[5], o[6], o[7]}; el[0] = lme; }
    BAR();
    f32x4 a, b; float l;
    if (par == 0) {
      a = ex[128] + (f32x4){o[0], o[1], o[2], o[3]}; b = ex[192] + (f32x4){o[4], o[5], o[6], o[7]}; l = lme + el[0];
    } else {
      a = ex[0] + (f32x4){o[8], o[9], o[10], o[11]}; b = ex[64] + (f32x4){o[12], o[13], o[14], o[15]}; l = b[0];
    }
    l = swapadd(h ? l : 0.f);
    const float rl = __builtin_amdgcn_rcpf(l);
    a *= rl; b *= rl;
    if (par && h) b = (f32x4){0.f, 0.f, 0.f, 0.f};
    if (pass == 0) {
      const float mx = fmaxf(fmaxf(fmaxf(fabsf(a[0]), fabsf(a[1])), fmaxf(fabsf(a[2]), fabsf(a[3]))), fmaxf(fmaxf(fabsf(b[0]), fabsf(b[1])), fmaxf(fabsf(b[2]), fabsf(b[3]))));
      const float sm = ((a[0] + a[1]) + (a[2] + a[3])) + ((b[0] + b[1]) + (b[2] + b[3]));
      const bool bad = !(l > 5.4e-20f && l < 1.8e19f) || !(mx < 3.0e38f) || (sm != sm);
      if (__any(bad) && lane == 0) *(volatile LDSP unsigned*)(cnt + 7) = 1u;
      BAR();
      if (__builtin_amdgcn_readfirstlane(*(volatile LDSP unsigned*)(cnt + 7))) {
        u32x4 q1z = q1; if (h) { q1z[2] = 0u; q1z[3] = 0u; }
        const unsigned kb0 = WS_KV + head * 262144 + lane * 16;
        float mrow = -INFINITY;
#pragma unroll 1
        for (int t = 0; t < 64; ++t) {
          f32x16 s = mfma32(asb(ldc(rs, kb0 + t * 4096)), qa, zero);
          s = mfma32(asb(ldc(rs, kb0 + t * 4096 + 1024)), asb(q1z), s);
#pragma unroll
          for (int r = 0; r < 16; ++r) mrow = fmaxf(mrow, s[r]);
        }
        auto rr = __builtin_amdgcn_permlane32_swap(__float_as_uint(mrow), __float_as_uint(mrow), false, false);
        mrow = fmaxf(__uint_as_float(rr[0]), __uint_as_float(rr[1]));
        o = zero;
#pragma unroll 1
        for (int t = par; t < 64; t += 2) {
          f32x16 s = mfma32(asb(ldc(rs, kb0 + t * 4096)), qa, zero);
          s = mfma32(asb(ldc(rs, kb0 + t * 4096 + 1024)), asb(q1z), s);
#pragma unroll
          for (int r = 0; r < 16; ++r) s[r] = EX2(s[r] - mrow);
          o = mfma32(asb(ldc(rs, kb0 + t * 4096 + 2048)), asb(pack8(s, 0)), o);
          o = mfma32(asb(ldc(rs, kb0 + t * 4096 + 3072)), asb(pack8(s, 8)), o);
        }
        continue;
      }
    }
    stc(rs, WS_O + (unsigned)(((jb * 4 + w4) * 16 + head) * 2 + par) * 1024 + lane * 16, pack44(a, b));
    break;
  }
  STAMP(7);
  if (dotail) {
    WAITV(1);
    BAR();
    STAMP(8);
    const unsigned oo = WS_O + (unsigned)((rbk * 2 + (w >> 2)) * 32 + w4 * 8) * 1024 + lane * 16;
    {
      const int cb = (w >> 2) * 64 + 32 * blk + 8 * p;
      const LDSP char* ya = L + L_Y1 + (((w >> 1) & 1) * 224 + (w & 1) * 112 + 8 * h + q) * 128 + ((((cb >> 4) ^ ((q >> 1) << 2)) & 7) << 4) + (cb & 8);
      f32x16 acc = {};
#pragma unroll
      for (int i = 0; i < 7; ++i) acc = mfma32(gf[i], cat(ldtr(ya + i * 2048), ldtr(ya + i * 2048 + 512)), acc);
      LDSP f32x4* ex = (LDSP f32x4*)(L + L_EX + w * 4096) + lane;
#pragma unroll
      for (int i = 0; i < 4; ++i) ex[i * 64] = (f32x4){acc[4 * i], acc[4 * i + 1], acc[4 * i + 2], acc[4 * i + 3]};
      WAITV(0);
      BAR();
      if (tid == 0) stc1(rs, FL_O + bid * 4, tag);
#pragma unroll
      for (int f = 0; f < 8; ++f) fx[f] = ldc(rs, oo + f * 1024);
      const int rt = w >> 2, sg = (w >> 1) & 1, kh = w & 1;
      const LDSP f32x4* e = (const LDSP f32x4*)(L + L_EX + rt * 16384 + kh * 2048) + lane;
      const f32x4 ea = e[0] + e[256], eb = e[64] + e[320], oa = e[512] + e[768], ob = e[576] + e[832];
      const float sc = 2.0f / (448.0f * 2048.0f);
      store_half(out, rbk * 64 + rt * 32, nb * 32 + sg * 224 + kh * 16, lane, (sg ? ea - oa : ea + oa) * sc, (sg ? eb - ob : eb + ob) * sc);
    }
#pragma unroll 1
    for (int spin = 0; spin < SPIN_CAP; ++spin) {
      unsigned m = 0;
#pragma unroll
      for (int f = 0; f < 8; ++f) m = max(max(m, max(fx[f][0], fx[f][1])), max(fx[f][2], fx[f][3]));
      if (__all(m != 0xffffffffu)) break;
      bool done = false;
      if (spin >= 16) {
        const int hh = w4 * 4 + (lane & 3), pj = (rbk * 2 + (w >> 2)) >> 2;
        done = __all(ldc1(rs, FL_O + ((hh >> 1) + 8 * ((hh & 1) + 2 * pj)) * 4) == tag);
        asm volatile("" ::: "memory");
      }
#pragma unroll
      for (int f = 0; f < 8; ++f) fx[f] = ldc(rs, oo + f * 1024);
      if (done) break;
    }
    STAMP(9);
    {
      const int cn = 32 * blk + 8 * p;
      const LDSP char* wa = L + L_WO + (w4 * 112 + 4 * h + q) * 128 + (cn & 8);
      const unsigned s0 = (((cn >> 4) ^ ((q >> 1) << 2)) & 7) << 4;
      const LDSP char* wa0 = wa + s0;
      const LDSP char* wa1 = wa + (s0 ^ 64);
      f32x16 acc0 = {}, acc1 = {};
#pragma unroll
      for (int i = 0; i < 8; ++i) {
        const int ro = ((i >> 1) * 28 + (i & 1) * 16) * 128;
        const int r2 = (i == 7) ? (h ? 512 : 1024) : 1024;
        acc0 = mfma32(cat(ldtr(wa0 + ro), ldtr(wa0 + ro + r2)), asb(fx[i]), acc0);
        acc1 = mfma32(cat(ldtr(wa1 + ro), ldtr(wa1 + ro + r2)), asb(fx[i]), acc1);
      }
      LDSP f32x4* ex = (LDSP f32x4*)(L + L_Y1 + par * 32768 + w4 * 2048) + lane;
      f32x4 a, b;
#pragma unroll
      for (int u = 0; u < 4; ++u) {
        const f32x16& ac = (u >> 1) ? acc1 : acc0;
        const int r0 = 8 * (u & 1);
        const f32x4 x = {ac[r0], ac[r0 + 1], ac[r0 + 2], ac[r0 + 3]}, y = {ac[r0 + 4], ac[r0 + 5], ac[r0 + 6], ac[r0 + 7]};
        if (u == w4) { a = x; b = y; } else { ex[u * 512] = x; ex[u * 512 + 64] = y; }
      }
      BAR();
      const LDSP f32x4* ox = (const LDSP f32x4*)(L + L_Y1 + par * 32768 + w4 * 8192) + lane;
#pragma unroll
      for (int u = 1; u < 4; ++u) { const int src = (w4 + u) & 3; a += ox[src * 128]; b += ox[src * 128 + 64]; }
      store_half(out, rbk * 64 + par * 32, 448 + nb * 64 + w4 * 16, lane, a, b);
    }
    if (tid == 0 && bid == 0) stc1(rs, WS_FL, tag);
  } else {
    WAITV(0);
    BAR();
    if (tid == 0) stc1(rs, FL_O + bid * 4, tag);
  }
  STAMP(10);
}

extern "C" void kernel_launch(void* const* d_in, const int* in_sizes, int n_in, void* d_out, int out_size, void* d_ws, size_t ws_size,
                              hipStream_t stream) {
  fused<<<256, 512, 0, stream>>>((const u16*)d_in[0], (const u16*)d_in[1], (const u16*)d_in[2], (const u16*)d_in[3], (const u16*)d_in[4],
                                 (const u16*)d_in[5], (u16*)d_out, (char*)d_ws);
}
```

```cpp
#include <hip/hip_runtime.h>
#include <stdint.h>
#include <type_traits>

typedef unsigned short u16;
typedef short bf16x8 __attribute__((ext_vector_type(8)));
typedef short s16x4 __attribute__((ext_vector_type(4)));
typedef float f32x16 __attribute__((ext_vector_type(16)));
typedef float f32x4 __attribute__((ext_vector_type(4)));
typedef float f32x2 __attribute__((ext_vector_type(2)));
typedef unsigned u32x4 __attribute__((ext_vector_type(4)));
typedef __bf16 bf16x2_t __attribute__((ext_vector_type(2)));
#define LDSP __attribute__((address_space(3)))
#define DI __device__ __forceinline__

constexpr int NF = 225;
constexpr unsigned WS_KV = 0;
constexpr unsigned WS_X1 = 4u << 20;
constexpr unsigned WS_WT = 6u << 20;
constexpr unsigned WS_Y1 = 8u << 20;
constexpr unsigned WS_O = 10u << 20;
constexpr unsigned WS_Q = 12u << 20;
constexpr unsigned WS_FL = 14u << 20;
constexpr unsigned WS_USED = WS_FL + 4096;
constexpr unsigned FL_Y = WS_FL, FL_O = WS_FL + 1024;
constexpr int L_WO = 0, L_Y1 = 57344, L_X = 114688, L_EX = 131072, L_T2 = 139264, L_FL = 147456, L_M = 148480, L_CNT = 152576;
constexpr int SPIN_CAP = 1 << 18;
constexpr int PF_LINES = 6;

DI unsigned pk2(float lo, float hi) { bf16x2_t v = __builtin_convertvector((f32x2){lo, hi}, bf16x2_t); return __builtin_bit_cast(unsigned, v); }
DI float bflo(unsigned u) { return __uint_as_float(u << 16); }
DI float bfhi(unsigned u) { return __uint_as_float(u & 0xffff0000u); }
DI f32x16 mfma32(bf16x8 a, bf16x8 b, f32x16 c) { return __builtin_amdgcn_mfma_f32_32x32x16_bf16(a, b, c, 0, 0, 0); }
DI f32x4 mfma16(bf16x8 a, bf16x8 b, f32x4 c) { return __builtin_amdgcn_mfma_f32_16x16x32_bf16(a, b, c, 0, 0, 0); }
DI bf16x8 asb(u32x4 v) { return __builtin_bit_cast(bf16x8, v); }
DI u32x4 asu(bf16x8 v) { return __builtin_bit_cast(u32x4, v); }
DI int phi(int rt, int rho) { return 8 * (rho >> 2) + 4 * rt + (rho & 3); }
DI float cosr(float rev) { return __builtin_amdgcn_cosf(rev); }
DI bf16x8 ldsr(const LDSP char* p) { return *(const LDSP bf16x8*)p; }
DI s16x4 ldtr(const LDSP char* p) { return __builtin_amdgcn_ds_read_tr16_b64_v4i16((LDSP s16x4*)p); }
DI bf16x8 cat(s16x4 a, s16x4 b) { return __builtin_shufflevector(a, b, 0, 1, 2, 3, 4, 5, 6, 7); }
DI float swapadd(float v) {
  auto r = __builtin_amdgcn_permlane32_swap(__float_as_uint(v), __float_as_uint(v), false, false);
  return __uint_as_float(r[0]) + __uint_as_float(r[1]);
}
DI u32x4 pack8(const f32x16& x, int o) { return (u32x4){pk2(x[o], x[o + 1]), pk2(x[o + 2], x[o + 3]), pk2(x[o + 4], x[o + 5]), pk2(x[o + 6], x[o + 7])}; }
DI u32x4 pack44(f32x4 a, f32x4 b) { return (u32x4){pk2(a[0], a[1]), pk2(a[2], a[3]), pk2(b[0], b[1]), pk2(b[2], b[3])}; }
DI void dma1(const void* base, unsigned voff, unsigned lds) {
  unsigned sv;
  asm volatile("s_mov_b32 %0, m0\n\ts_mov_b32 m0, %3\n\ts_nop 0\n\tglobal_load_lds_dwordx4 %1, %2\n\ts_mov_b32 m0, %0"
               : "=&s"(sv) : "v"(voff), "s"(base), "s"(lds) : "memory");
}
DI void dma1c(const void* base, unsigned voff, unsigned lds) {
  unsigned sv;
  asm volatile("s_mov_b32 %0, m0\n\ts_mov_b32 m0, %3\n\ts_nop 0\n\tglobal_load_lds_dwordx4 %1, %2 sc1\n\ts_mov_b32 m0, %0"
               : "=&s"(sv) : "v"(voff), "s"(base), "s"(lds) : "memory");
}
DI void touch(const void* base, unsigned voff, unsigned lds) {
  unsigned sv;
  asm volatile("s_mov_b32 %0, m0\n\ts_mov_b32 m0, %3\n\ts_nop 0\n\tglobal_load_lds_dword %1, %2\n\ts_mov_b32 m0, %0"
               : "=&s"(sv) : "v"(voff), "s"(base), "s"(lds) : "memory");
}
template <int OFF> DI void dmao(const void* base, unsigned voff, unsigned lds) {
  unsigned sv;
  asm volatile("s_mov_b32 %0, m0\n\ts_mov_b32 m0, %3\n\ts_nop 0\n\tglobal_load_lds_dwordx4 %1, %2 offset:%4\n\ts_mov_b32 m0, %0"
               : "=&s"(sv) : "v"(voff), "s"(base), "s"(lds), "i"(OFF) : "memory");
}
#define WAITV(N) asm volatile("s_waitcnt vmcnt(" #N ")" ::: "memory")
#define BAR() asm volatile("s_waitcnt lgkmcnt(0)\n\ts_barrier" ::: "memory")
#define SB() __builtin_amdgcn_sched_barrier(0)
#define RS __amdgpu_buffer_rsrc_t
DI unsigned ldc1(RS r, unsigned off) { return __builtin_amdgcn_raw_buffer_load_b32(r, off, 0, 16); }
DI void stc(RS r, unsigned off, u32x4 v) { __builtin_amdgcn_raw_buffer_store_b128(v, r, off, 0, 16); }
DI void stc1(RS r, unsigned off, unsigned v) { __builtin_amdgcn_raw_buffer_store_b32(v, r, off, 0, 16); }
DI unsigned lds_arrive(LDSP unsigned* c) {
  unsigned old = 0;
  if ((threadIdx.x & 63) == 0) old = __hip_atomic_fetch_add(c, 1u, __ATOMIC_RELAXED, __HIP_MEMORY_SCOPE_WORKGROUP);
  return __builtin_amdgcn_readfirstlane(old);
}
DI void lds_inc(unsigned addr) {
  asm volatile("s_mov_b64 exec, 1\n\tds_add_u32 %0, %1\n\ts_mov_b64 exec, -1" :: "v"(addr), "v"(1u) : "memory");
}
DI void lds_wait(LDSP unsigned* c, unsigned n) {
  unsigned v, sv, k;
  asm volatile("s_mov_b32 %2, 0x400000\n1:\n\tds_read_b32 %0, %3\n\ts_waitcnt lgkmcnt(0)\n\tv_readfirstlane_b32 %1, %0\n\ts_nop 0\n\ts_cmp_ge_u32 %1, %4\n\ts_cbranch_scc1 2f\n\t"
               "s_sleep 2\n\ts_sub_u32 %2, %2, 1\n\ts_cbranch_scc0 1b\n2:"
               : "=&v"(v), "=&s"(sv), "=&s"(k) : "v"((unsigned)(uintptr_t)c), "s"(n) : "memory", "scc");
}
DI void lds_waits(LDSP unsigned* c, unsigned n) {
  unsigned v, sv, k;
  asm volatile("s_mov_b32 %2, 0x100000\n1:\n\tds_read_b32 %0, %3\n\ts_waitcnt lgkmcnt(0)\n\tv_readfirstlane_b32 %1, %0\n\ts_nop 0\n\ts_cmp_ge_u32 %1, %4\n\ts_cbranch_scc1 2f\n\t"
               "s_sleep 12\n\ts_sub_u32 %2, %2, 1\n\ts_cbranch_scc0 1b\n2:"
               : "=&v"(v), "=&s"(sv), "=&s"(k) : "v"((unsigned)(uintptr_t)c), "s"(n) : "memory", "scc");
}
DI void store_half(RS ro, int row0, int col0, int lane, f32x4 a, f32x4 b) {
  unsigned a0 = pk2(a[0], a[1]), a1 = pk2(a[2], a[3]), b0 = pk2(b[0], b[1]), b1 = pk2(b[2], b[3]);
  auto s0 = __builtin_amdgcn_permlane32_swap(a0, b0, false, false);
  auto s1 = __builtin_amdgcn_permlane32_swap(a1, b1, false, false);
  __builtin_amdgcn_raw_buffer_store_b128((u32x4){s0[0], s1[0], s0[1], s1[1]}, ro, (unsigned)(((row0 + (lane & 31)) * 896 + col0) * 2 + (lane >> 5) * 16), 0, 0);
}
#define PIN(x) asm volatile("" : "+v"(x))
#define EX2(v) __builtin_amdgcn_exp2f(v)

typedef float f32x8 __attribute__((ext_vector_type(8)));
DI bf16x8 pack_f8(const f32x8& v) { return asb((u32x4){pk2(v[0], v[1]), pk2(v[2], v[3]), pk2(v[4], v[5]), pk2(v[6], v[7])}); }

__global__ __launch_bounds__(512, 2) void k1(const u16* __restrict__ ax, const u16* __restrict__ wr, const u16* __restrict__ wi,
                                             const u16* __restrict__ bx, const u16* __restrict__ wqkv, const u16* __restrict__ wo, const u16* __restrict__ out, char* __restrict__ ws) {
  __shared__ __attribute__((aligned(1024))) char smem[148480];
  LDSP char* L = (LDSP char*)smem;
  const unsigned lds0 = (unsigned)(uintptr_t)L;
  const int tid = threadIdx.x, lane = tid & 63, w = __builtin_amdgcn_readfirstlane(tid >> 6);
  const int r32 = lane & 31, h = lane >> 5, w4 = w & 3;
  const int xcd = blockIdx.x & 7, idx = blockIdx.x >> 3;
  const unsigned xsw = (unsigned)((r32 * 128) + (((h ^ (r32 >> 1)) & 7) << 4));
  const RS rs = __builtin_amdgcn_make_buffer_rsrc(ws, 0, WS_USED, 0x00020000);
  if (w < 3) {
    if (w == 0) { if (lane < PF_LINES) touch((const char*)(__builtin_amdgcn_s_getpc() & ~127ull), (idx * PF_LINES + lane) * 128, lds0 + 147456); }
    else if (lane < 49) touch(wo, (idx * 98 + (w - 1) * 49 + lane) * 128, lds0 + 147456 + w * 256);
    if (w == 1 && lane < 7) touch(out, lane * 524288, lds0 + 147456 + 768);
  }
  if (idx < 24) {
    const int rb = (xcd & 1) * 8 + (idx & 7), part = idx >> 3, grp = xcd >> 1;
    LDSP unsigned* cnt = (LDSP unsigned*)(L + 131072);
    if (w >= 4) {
      const unsigned xv0 = (unsigned)((32 * w4 + (lane >> 3)) * 896 + (((lane & 7) ^ (lane >> 4)) << 4));
      const unsigned xv1 = (unsigned)((32 * w4 + 8 + (lane >> 3)) * 896 + (((lane & 7) ^ (4 + (lane >> 4))) << 4));
      const int c16 = min((lane & 15) ^ ((lane >> 4) << 2), (grp & 1) ? 15 : 13);
      const unsigned wv = (unsigned)((16 * w4 + (lane >> 4)) * 2688 + 896 * part + ((224 * grp) & ~63) + c16 * 16);
      const char* xb = (const char*)bx + (size_t)rb * (128 * 896);
      const int rot = (idx * 3 + xcd) % 7;
      auto issue = [&](int cs) {
        const unsigned d = lds0 + (cs & 3) * 32768 + w4 * 4096;
        const int c = cs + rot >= 7 ? cs + rot - 7 : cs + rot;
#pragma unroll
        for (int i = 0; i < 4; ++i) dma1((const char*)wqkv + (64 * c + 4 * i) * 2688, wv, d + 16384 + i * 1024);
#pragma unroll
        for (int i = 0; i < 4; ++i) dma1(xb + c * 128 + (i >> 1) * (16 * 896), (i & 1) ? xv1 : xv0, d + i * 1024);
      };
      issue(0);
      if (lane < 16) cnt[lane] = 0u;
      BAR();
      issue(1); issue(2);
#pragma unroll
      for (int c = 0; c < 7; ++c) {
        if (c <= 4) WAITV(16); else if (c == 5) WAITV(10); else WAITV(2);
        lds_inc(lds0 + 131072 + 4 * c);
        if (c >= 1 && c < 4) lds_wait(cnt + 8 + c - 1, 4);
        if (c < 4) issue(c + 3);
        if (c == 4) { stc(rs, WS_O + blockIdx.x * 8192 + w4 * 2048 + lane * 16, (u32x4){~0u, ~0u, ~0u, ~0u}); stc(rs, WS_O + blockIdx.x * 8192 + w4 * 2048 + 1024 + lane * 16, (u32x4){~0u, ~0u, ~0u, ~0u}); }
      }
      return;
    }
    BAR();
    const int wr2 = w >> 1, wc = w & 1;
    const int blk = (lane >> 4) & 1, q = (lane & 15) >> 2, p = lane & 3;
    const int b0 = (grp & 1) * 32 + 112 * wc + 32 * blk + 8 * p, b1 = b0 + 56;
    const unsigned wa0 = 16384 + (8 * h + q) * 256 + ((((b0 >> 4) ^ (q << 2)) & 15) << 4) + (b0 & 8);
    const unsigned wa1 = 16384 + (8 * h + q) * 256 + ((((b1 >> 4) ^ (q << 2)) & 15) << 4) + (b1 & 8);
    f32x16 acc[2][2] = {};
    auto gemm = [&](auto vtag) {
      constexpr bool V = decltype(vtag)::value;
      bf16x8 xa[2][2], wa[2][2], xb_[2][2], wb[2][2];
      auto rd = [&](const LDSP char* sb, int k0, bf16x8 (&x)[2][2], bf16x8 (&wq)[2][2]) {
#pragma unroll
        for (int kk = 0; kk < 2; ++kk) {
          const LDSP char* xp = sb + wr2 * 8192 + (xsw ^ ((k0 + kk) << 5));
          x[kk][0] = ldsr(xp); x[kk][1] = ldsr(xp + 4096);
          wq[kk][0] = cat(ldtr(sb + wa0 + (k0 + kk) * 4096), ldtr(sb + wa0 + (k0 + kk) * 4096 + 1024));
          wq[kk][1] = cat(ldtr(sb + wa1 + (k0 + kk) * 4096), ldtr(sb + wa1 + (k0 + kk) * 4096 + 1024));
        }
      };
      auto mmrd = [&](bf16x8 (&x)[2][2], bf16x8 (&wq)[2][2], const LDSP char* sb, int k0, bf16x8 (&xn)[2][2], bf16x8 (&wn)[2][2]) {
        s16x4 t[8];
#pragma unroll
        for (int e = 0; e < 8; ++e) {
          const int kk = e >> 2, i = (e >> 1) & 1, j = e & 1;
          acc[i][j] = V ? mfma32(x[kk][i], wq[kk][j], acc[i][j]) : mfma32(wq[kk][j], x[kk][i], acc[i][j]);
          if (e & 1) xn[e >> 2][(e >> 1) & 1] = ldsr(sb + wr2 * 8192 + (xsw ^ ((k0 + (e >> 2)) << 5)) + ((e >> 1) & 1) * 4096);
          t[e] = ldtr(sb + ((e & 2) ? wa1 : wa0) + (k0 + (e >> 2)) * 4096 + (e & 1) * 1024);
          SB();
        }
#pragma unroll
        for (int e = 0; e < 4; ++e) wn[e >> 1][e & 1] = cat(t[2 * e], t[2 * e + 1]);
      };
      lds_wait(cnt, 4);
      rd(L, 0, xa, wa);
#pragma unroll 1
      for (int c = 0; c < 7; ++c) {
        const unsigned pk = *(const volatile LDSP unsigned*)(cnt + c + 1);
        SB();
        mmrd(xa, wa, L + (c & 3) * 32768, 2, xb_, wb);
        lds_inc(lds0 + 131072 + 32 + 4 * c);
        const bool rdy = __builtin_amdgcn_readfirstlane(pk) >= 4;
        mmrd(xb_, wb, L + ((c + 1) & 3) * 32768, 0, xa, wa);
        if (!rdy && c < 6) { lds_wait(cnt + c + 1, 4); rd(L + ((c + 1) & 3) * 32768, 0, xa, wa); }
      }
    };
    if (part == 2) gemm(std::true_type{}); else gemm(std::false_type{});
    const float sc = part == 0 ? 0.18898223650461362f * 1.4426950408889634f : 1.0f;
#pragma unroll
    for (int i = 0; i < 2; ++i)
#pragma unroll
      for (int j = 0; j < 2; ++j) {
        const int head = grp * 4 + 2 * wc + j, t = rb * 4 + 2 * wr2 + i;
        const f32x16 v = acc[i][j] * sc;
        u32x4 a = pack8(v, 0), b = pack8(v, 8);
        if (part == 2) { if (r32 >= 28) { const unsigned f = r32 == 28 ? 0x3f803f80u : 0u; a = (u32x4){f, f, f, f}; b = a; } }
        else if (h) { b[2] = part ? 0x00003f80u : 0u; b[3] = 0u; }
        const unsigned o = (part == 0 ? WS_Q + (unsigned)(head * 64 + t) * 2048 : WS_KV + (unsigned)(head * 64 + t) * 4096 + (part - 1) * 2048) + lane * 16;
        stc(rs, o, a); stc(rs, o + 1024, b);
      }
    return;
  }
  const int fw = xcd * 8 + idx - 24, m = fw >> 2, kg = fw & 3;
  {
#pragma unroll
    for (int i = 0; i < 2; ++i) {
      const unsigned fo = min((unsigned)((w * 2 + i) * 1024 + lane * 16), 14384u);
      dma1((const char*)wr + fw * 14400, fo, lds0 + 114688 + (w * 2 + i) * 1024);
      dma1((const char*)wi + fw * 14400, fo, lds0 + 131072 + (w * 2 + i) * 1024);
    }
    const int rt = w >> 1;
#pragma unroll
    for (int i = 0; i < 2; ++i) {
      const int rr = 16 * (w & 1) + 8 * i + (lane >> 3);
      const unsigned av = (unsigned)((32 * (32 * (rt & 1) + rr) + 2 * m + (rt >> 1)) * 896 + (((lane & 7) ^ ((rr >> 1) & 7)) << 4));
#pragma unroll
      for (int c = 0; c < 7; ++c) dma1((const char*)ax + c * 128, av, lds0 + rt * 28672 + c * 4096 + (2 * (w & 1) + i) * 1024);
    }
  }
  bf16x8 tab[14];
  {
    const float kf = (float)(32 * kg + r32) * (1.0f / 448.0f);
    const float fa0 = __builtin_fmaf((float)(8 * h + (w4 & 1)), kf, (w4 & 2) ? -0.25f : 0.f);
    f32x8 u0, u1;
#pragma unroll
    for (int j = 0; j < 8; ++j) { u0[j] = cosr(__builtin_fmaf(kf, (float)(2 * (j & 3) + 16 * (j >> 2)), fa0)); u1[j] = cosr(__builtin_fmaf(kf, (float)(32 + 2 * (j & 3) + 16 * (j >> 2)), fa0)); }
    const float tc = 2.f * cosr(32.f * kf);
    tab[0] = pack_f8(u0); tab[1] = pack_f8(u1);
#pragma unroll
    for (int n = 2; n < 14; ++n) { const f32x8 u2 = u1 * tc - u0; u0 = u1; u1 = u2; tab[n] = pack_f8(u2); }
#pragma unroll
    for (int n = 0; n < 14; ++n) PIN(tab[n]);
  }
  WAITV(14);
  BAR();
  u32x4 wt[4];
#pragma unroll
  for (int i = 0; i < 4; ++i) {
    const int it = min(i * 512 + tid, 1799), k = it >> 3, c = it & 7;
    const LDSP u16* a = (const LDSP u16*)(L + 114688 + c * 1800) + k;
    const LDSP u16* b = (const LDSP u16*)(L + 131072 + c * 1800) + k;
    wt[i] = (u32x4){a[0] | ((unsigned)b[0] << 16), a[225] | ((unsigned)b[225] << 16), a[450] | ((unsigned)b[450] << 16), a[675] | ((unsigned)b[675] << 16)};
    PIN(wt[i]);
  }
  WAITV(0);
  BAR();
#pragma unroll
  for (int i = 0; i < 4; ++i) { const int it = i * 512 + tid; if (it < 1800) stc(rs, WS_WT + (it >> 3) * 8192 + fw * 128 + (it & 7) * 16, wt[i]); }
  f32x16 acc[2] = {};
  {
    const unsigned sel = (w4 & 1) ? 0x07060302u : 0x05040100u;
    const LDSP char* xp = L + (w >> 2) * 57344;
    u32x4 xa[2][8];
    auto ldx = [&](int c) {
#pragma unroll
      for (int i = 0; i < 8; ++i) xa[c & 1][i] = asu(ldsr(xp + (i >> 2) * 28672 + c * 4096 + (xsw ^ ((i & 3) << 5))));
    };
    ldx(0);
#pragma unroll
    for (int c = 0; c < 7; ++c) {
      if (c < 6) ldx(c + 1);
      SB();
#pragma unroll
      for (int i = 0; i < 4; ++i) {
        const int t = i & 1, uu = i >> 1;
        const u32x4 a = xa[c & 1][4 * t + 2 * uu], b = xa[c & 1][4 * t + 2 * uu + 1];
        const u32x4 e = {__builtin_amdgcn_perm(a[1], a[0], sel), __builtin_amdgcn_perm(a[3], a[2], sel), __builtin_amdgcn_perm(b[1], b[0], sel), __builtin_amdgcn_perm(b[3], b[2], sel)};
        acc[t] = mfma32(asb(e), tab[2 * c + uu], acc[t]);
      }
      SB();
    }
  }
  stc(rs, WS_O + blockIdx.x * 8192 + tid * 16, (u32x4){~0u, ~0u, ~0u, ~0u});
  if (fw == 0 && w < 2) stc(rs, WS_FL + w * 1024 + lane * 16, (u32x4){0u, 0u, 0u, 0u});
  BAR();
#pragma unroll
  for (int t = 0; t < 2; ++t) {
    LDSP f32x4* ex = (LDSP f32x4*)(L + ((w >> 2) * 2 + t) * 16384 + w4 * 4096) + lane;
#pragma unroll
    for (int i = 0; i < 4; ++i) ex[i * 64] = (f32x4){acc[t][4 * i], acc[t][4 * i + 1], acc[t][4 * i + 2], acc[t][4 * i + 3]};
  }
  BAR();
#pragma unroll
  for (int t = 0; t < 2; ++t) {
    const int rt = (w >> 2) * 2 + t, kl = w4 * 8 + (lane >> 3), pc = lane & 7, k = 32 * kg + kl;
    const LDSP f32x4* e = (const LDSP f32x4*)(L + rt * 16384 + (pc >> 1) * 1024 + (kl + 32 * (pc & 1)) * 16);
    const f32x4 ce = e[0], co = e[256], se = e[512], so = e[768];
    const unsigned xo = WS_X1 + (2 * m + (rt >> 1)) * 256 + (rt & 1) * 128 + pc * 16;
    if (k <= 112) stc(rs, xo + k * 8192, pack44(ce + co, -se - so));
    if (k < 112) stc(rs, xo + (224 - k) * 8192, pack44(ce - co, se - so));
  }
}

DI bf16x8 ldps(RS r, unsigned voff, unsigned soff) { return asb(__builtin_amdgcn_raw_buffer_load_b128(r, voff, soff, 0)); }
__global__ __launch_bounds__(512, 2) void k2(const u16* __restrict__ wo, u16* __restrict__ out, char* __restrict__ ws) {
  __shared__ __attribute__((aligned(1024))) char smem[153600];
  LDSP char* L = (LDSP char*)smem;
  const unsigned lds0 = (unsigned)(uintptr_t)L;
  const int tid = threadIdx.x, lane = tid & 63, w = __builtin_amdgcn_readfirstlane(tid >> 6);
  const int r32 = lane & 31, h = lane >> 5, l16 = lane & 15, g = lane >> 4;
  const int bid = blockIdx.x, xcd = bid & 7, bi = bid >> 3, head = 2 * xcd + (bi & 1), jb = bi >> 1;
  const int w4 = w & 3, par = w >> 2;
  const unsigned v16 = lane * 16;
  constexpr unsigned tag = 1u;
  const RS rs = __builtin_amdgcn_make_buffer_rsrc(ws, 0, WS_USED, 0x00020000);
  const RS ro = __builtin_amdgcn_make_buffer_rsrc(out, 0, 2048 * 896 * 2, 0x00020000);
  const bool dotail = bi < 28;
  const int rbk = xcd * 4 + (bi & 3), nb = dotail ? bi >> 2 : 6;
  const int blk = (lane >> 4) & 1, q = l16 >> 2, p = lane & 3;
  const f32x16 zero = {};
  LDSP unsigned* cnt = (LDSP unsigned*)(L + L_CNT);
  if (tid < 16) cnt[tid] = 0u;
  BAR();
  if (w < 4) {
    __builtin_amdgcn_s_setprio(1);
    const unsigned kv0 = WS_KV + head * 262144 + w * 4096;
    auto kvo = [&](int n) { return kv0 + (((jb & 8) + n) & 15) * 16384; };
    bf16x8 ka[2], kb[2], va[2], vb[2], qa[4];
    u32x4 qb[4];
    ka[0] = ldps(rs, v16, kvo(0)); kb[0] = ldps(rs, v16 + 1024, kvo(0));
#pragma unroll
    for (int j = 0; j < 4; ++j) { const unsigned qo = WS_Q + (head * 64 + jb * 4 + ((w + 1 + j) & 3)) * 2048; qa[j] = ldps(rs, v16, qo); qb[j] = asu(ldps(rs, v16 + 1024, qo)); }
    va[0] = ldps(rs, v16 + 2048, kvo(0)); vb[0] = ldps(rs, v16 + 3072, kvo(0));
    vb[1] = (bf16x8){};
    {
      f32x16 s = mfma32(ka[0], qa[3], zero); s = mfma32(kb[0], asb(qb[3]), s);
      float mx = fmaxf(s[0], s[1]);
#pragma unroll
      for (int r = 2; r < 16; r += 2) mx = fmaxf(fmaxf(s[r], s[r + 1]), mx);
      auto rr = __builtin_amdgcn_permlane32_swap(__float_as_uint(mx), __float_as_uint(mx), false, false);
      const unsigned mb = pk2(-fmaxf(__uint_as_float(rr[0]), __uint_as_float(rr[1])), 0.f);
      *(LDSP unsigned*)(L + L_M + w * 256 + lane * 4) = mb;
      lds_inc(lds0 + L_CNT + 56);
      ka[1] = ldps(rs, v16, kvo(1)); kb[1] = ldps(rs, v16 + 1024, kvo(1));
      va[1] = ldps(rs, v16 + 2048, kvo(1));
      lds_wait(cnt + 14, 4);
#pragma unroll
      for (int j = 0; j < 4; ++j) { const unsigned m = *(const LDSP unsigned*)(L + L_M + ((w + 1 + j) & 3) * 256 + lane * 4); if (h) qb[j][2] = m; }
    }
    f32x16 o[4] = {}, sA, sB;
    f32x4 ea, eb = {};
    u32x4 p0, p1 = {};
    sA = mfma32(ka[0], qa[0], zero); sA = mfma32(kb[0], asb(qb[0]), sA);
#define E4(d, S, i) d[0] = EX2(S[i]); d[1] = EX2(S[i + 1]); d[2] = EX2(S[i + 2]); d[3] = EX2(S[i + 3]); PIN(d);
#define C2(pp, i, e) pp[i] = pk2(e[0], e[1]); pp[i + 1] = pk2(e[2], e[3]);
#define TILE(SC, SN, KA, KB, JN, OP, VBP, OC, VA, H0, H1, H2, H3) {                                  \
    SN = mfma32(KA, qa[JN], zero); H0; SB(); C2(p1, 2, eb) SB(); E4(ea, SC, 0) SB();                     \
    OP = mfma32(VBP, asb(p1), OP); H1; SB(); C2(p0, 0, ea) SB(); E4(eb, SC, 4) SB();                     \
    SN = mfma32(KB, asb(qb[JN]), SN); H2; SB(); C2(p0, 2, eb) SB(); E4(ea, SC, 8) SB();                  \
    OC = mfma32(VA, asb(p0), OC); H3; SB(); C2(p1, 0, ea) SB(); E4(eb, SC, 12) SB(); }
#define KTILE(c, x, n) {        \
    const unsigned o1 = kvo((n) + 1), o2 = kvo((n) + 2);                                                 \
    TILE(sA, sB, ka[c], kb[c], 1, o[3], vb[x], o[0], va[c], , vb[x] = ldps(rs, v16 + 3072, o1), , )      \
    TILE(sB, sA, ka[c], kb[c], 2, o[0], vb[c], o[1], va[c], , , , )                                      \
    TILE(sA, sB, ka[c], kb[c], 3, o[1], vb[c], o[2], va[c], ka[c] = ldps(rs, v16, o2), , kb[c] = ldps(rs, v16 + 1024, o2), ) \
    TILE(sB, sA, ka[x], kb[x], 0, o[2], vb[c], o[3], va[c], , , , va[c] = ldps(rs, v16 + 2048, o2)) }
#pragma unroll 1
    for (int n = 0; n < 16; n += 2) {
      KTILE(0, 1, n)
      KTILE(1, 0, n + 1)
      if (n == 4 && w == 0) lds_inc(lds0 + L_CNT + 12);
    }
    C2(p1, 2, eb)
    o[3] = mfma32(vb[1], asb(p1), o[3]);
    __builtin_amdgcn_s_setprio(3);
    lds_wait(cnt + 2, 4);
#pragma unroll
    for (int j = 0; j < 3; ++j) {
      LDSP f32x4* d = (LDSP f32x4*)(L + L_WO + (((w + 1 + j) & 3) * 3 + j) * 4096) + lane;
#pragma unroll
      for (int i = 0; i < 4; ++i) d[i * 64] = (f32x4){o[j][4 * i], o[j][4 * i + 1], o[j][4 * i + 2], o[j][4 * i + 3]};
    }
    const bf16x8 qaw = qa[3];
    u32x4 qbw = qb[3];
    lds_inc(lds0 + L_CNT + 16);
    lds_wait(cnt + 4, 4);
    f32x16 acc = o[3];
#pragma unroll
    for (int j = 0; j < 3; ++j) {
      const LDSP f32x4* sp = (const LDSP f32x4*)(L + L_WO + (w * 3 + j) * 4096) + lane;
      f32x16 v;
#pragma unroll
      for (int i = 0; i < 4; ++i) { const f32x4 x = sp[i * 64]; v[4 * i] = x[0]; v[4 * i + 1] = x[1]; v[4 * i + 2] = x[2]; v[4 * i + 3] = x[3]; }
      acc += v;
    }
    float l = swapadd(h ? acc[12] : 0.f);
    {
      float mx = 0.f;
#pragma unroll
      for (int r = 0; r < 16; r += 2) mx = fmaxf(fmaxf(fabsf(acc[r]), fabsf(acc[r + 1])), mx);
      if (__builtin_expect(__any(!(mx < 1.0e38f) || !(l < 1.8e19f) || !(l > 5.4e-20f)), 0)) {
        if (h) { qbw[2] = 0u; qbw[3] = 0u; }
        const unsigned kb0 = WS_KV + head * 262144;
        float mrow = -INFINITY;
#pragma unroll 1
        for (int tt = 0; tt < 64; ++tt) {
          f32x16 s = mfma32(ldps(rs, v16, kb0 + tt * 4096), qaw, zero);
          s = mfma32(ldps(rs, v16 + 1024, kb0 + tt * 4096), asb(qbw), s);
#pragma unroll
          for (int r = 0; r < 16; ++r) mrow = fmaxf(mrow, s[r]);
        }
        auto rr = __builtin_amdgcn_permlane32_swap(__float_as_uint(mrow), __float_as_uint(mrow), false, false);
        mrow = fmaxf(__uint_as_float(rr[0]), __uint_as_float(rr[1]));
        acc = zero;
#pragma unroll 1
        for (int tt = 0; tt < 64; ++tt) {
          f32x16 s = mfma32(ldps(rs, v16, kb0 + tt * 4096), qaw, zero);
          s = mfma32(ldps(rs, v16 + 1024, kb0 + tt * 4096), asb(qbw), s);
#pragma unroll
          for (int r = 0; r < 16; ++r) s[r] = EX2(s[r] - mrow);
          acc = mfma32(ldps(rs, v16 + 2048, kb0 + tt * 4096), asb(pack8(s, 0)), acc);
          acc = mfma32(ldps(rs, v16 + 3072, kb0 + tt * 4096), asb(pack8(s, 8)), acc);
        }
        l = swapadd(h ? acc[12] : 0.f);
      }
    }
    acc *= __builtin_amdgcn_rcpf(l);
    u32x4 hi8 = pack8(acc, 8);
    if (h) { hi8[2] = 0u; hi8[3] = 0u; }
    const unsigned oo = WS_O + (unsigned)((jb * 4 + w) * 16 + head) * 2048 + v16;
    stc(rs, oo, pack8(acc, 0)); stc(rs, oo + 1024, hi8);
    __builtin_amdgcn_s_setprio(0);
    bf16x8 gf[14];
    {
      const float gdf = (float)(nb * 32 + r32) * (1.0f / 448.0f);
      const float ga0 = gdf * (float)(8 * h + (w4 & 1)), ga1 = ga0 + 0.25f;
      f32x8 u0, u1;
#pragma unroll
      for (int j = 0; j < 8; ++j) { u0[j] = cosr(__builtin_fmaf(gdf, (float)(j & ~1), (j & 1) ? ga1 : ga0)); u1[j] = cosr(__builtin_fmaf(gdf, (float)(16 + (j & ~1)), (j & 1) ? ga1 : ga0)); }
      const float tc = 2.f * cosr(16.f * gdf);
      gf[1] = pack_f8(u1);
      { f32x8 v = u0; if ((w4 & 1) == 0 && h == 0) { v[0] = 0.5f; v[1] = (r32 & 1) ? -0.5f : 0.5f; } gf[0] = pack_f8(v); }
#pragma unroll
      for (int n = 2; n < 14; ++n) { const f32x8 u2 = u1 * tc - u0; u0 = u1; u1 = u2; gf[n] = pack_f8(u2); }
#pragma unroll
      for (int n = 0; n < 14; ++n) PIN(gf[n]);
    }
    WAITV(0);
    if (lds_arrive(cnt + 10) == 3 && lane == 0) stc1(rs, FL_O + bid * 4, tag);
    if (dotail) {
      lds_wait(cnt + 8, 4);
      f32x16 acy = {}, acz = {};
      const int cb = (w4 >> 1) * 64 + 32 * blk + 8 * p;
      const LDSP char* ya = L + L_Y1 + ((w4 & 1) * 224 + 8 * h + q) * 128 + ((((cb >> 4) ^ ((q >> 1) << 2)) & 7) << 4) + (cb & 8);
#pragma unroll
      for (int i = 0; i < 14; i += 2) {
        acy = mfma32(gf[i], cat(ldtr(ya + i * 2048), ldtr(ya + i * 2048 + 512)), acy);
        acz = mfma32(gf[i + 1], cat(ldtr(ya + i * 2048 + 2048), ldtr(ya + i * 2048 + 2560)), acz);
      }
      acy += acz;
      LDSP f32x4* ex = (LDSP f32x4*)(L + L_X + w4 * 4096) + lane;
#pragma unroll
      for (int i = 0; i < 4; ++i) ex[i * 64] = (f32x4){acy[4 * i], acy[4 * i + 1], acy[4 * i + 2], acy[4 * i + 3]};
      lds_inc(lds0 + L_CNT + 36);
      lds_wait(cnt + 9, 4);
      const LDSP f32x4* e = (const LDSP f32x4*)(L + L_X + (w4 ^ 1) * 4096) + lane;
      const float sc = 2.0f / (448.0f * 2048.0f), so = (w4 & 1) ? -sc : sc;
#pragma unroll
      for (int kh = 0; kh < 2; ++kh) {
        const f32x4 a = {acy[8 * kh], acy[8 * kh + 1], acy[8 * kh + 2], acy[8 * kh + 3]}, b = {acy[8 * kh + 4], acy[8 * kh + 5], acy[8 * kh + 6], acy[8 * kh + 7]};
        store_half(ro, rbk * 64 + (w4 >> 1) * 32, nb * 32 + (w4 & 1) * 224 + kh * 16, lane, e[kh * 128] * sc + a * so, e[kh * 128 + 64] * sc + b * so);
      }
    }
  } else {
    __builtin_amdgcn_s_setprio(2);
    const bool dofft = bid < NF;
    const int kd = dofft ? bid : 224;
    auto gen_t = [&](float af, float a0, int b, bf16x8& f0, bf16x8& f1) {
      float c[4], sn[4];
#pragma unroll
      for (int r = 0; r < 4; ++r) { const float an = __builtin_fmaf(af, (float)(b + r), a0); c[r] = cosr(an); sn[r] = __builtin_amdgcn_sinf(an); }
      const unsigned c0 = pk2(c[0], c[1]), c1 = pk2(c[2], c[3]);
      f0 = asb((u32x4){c0, c1, pk2(sn[0], sn[1]), pk2(sn[2], sn[3])});
      f1 = asb((u32x4){pk2(-sn[0], -sn[1]), pk2(-sn[2], -sn[3]), c0, c1});
    };
    {
      {
        bf16x8 t2a, t2b;
        const float af = (float)phi(w4 >> 1, l16) * (1.0f / 32.0f);
        gen_t(af, af * (float)(8 * g), 4 * (w4 & 1), t2a, t2b);
        *((LDSP bf16x8*)(L + L_T2 + w4 * 2048) + lane) = t2a;
        *((LDSP bf16x8*)(L + L_T2 + w4 * 2048 + 1024) + lane) = t2b;
      }
      bf16x8 t1[8];
      f32x4 tw[8];
      const float taf = (float)(16 * w4 + l16) * (1.0f / 64.0f), ta0 = taf * (float)(4 * g);
#pragma unroll
      for (int c = 0; c < 4; ++c) { gen_t(taf, ta0, 16 * c, t1[2 * c], t1[2 * c + 1]); PIN(t1[2 * c]); PIN(t1[2 * c + 1]); }
#pragma unroll
      for (int e = 0; e < 4; ++e) {
        const int dir = e >> 1, rt = e & 1;
#pragma unroll
        for (int r = 0; r < 4; ++r) {
          const int n2 = dir ? phi(rt, l16) : phi(rt, 4 * g + r), k1 = dir ? 16 * w4 + 4 * g + r : 16 * w4 + l16;
          const float an = (float)(n2 * k1) * (1.0f / 2048.0f);
          tw[2 * e][r] = cosr(an); tw[2 * e + 1][r] = __builtin_amdgcn_sinf(an);
        }
        PIN(tw[2 * e]); PIN(tw[2 * e + 1]);
      }
      const unsigned xv = (lane & 48) * 16 + (((lane & 15) ^ (w4 * 4 + g)) << 4);
    dmao<0>(ws + WS_X1 + kd * 8192 + w4 * 2048, xv, lds0 + L_X + w4 * 2048);
    dmao<1024>(ws + WS_X1 + kd * 8192 + w4 * 2048, xv, lds0 + L_X + w4 * 2048);
    dma1(ws + WS_WT + kd * 8192 + w4 * 64, (lane >> 2) * 256 + (lane & 3) * 16, lds0 + L_EX + w4 * 2048);
    dma1(ws + WS_WT + kd * 8192 + 4096 + w4 * 64, (lane >> 2) * 256 + (lane & 3) * 16, lds0 + L_EX + w4 * 2048 + 1024);
      __builtin_amdgcn_s_setprio(0);
      WAITV(0);
      lds_inc(lds0 + L_CNT);
      lds_waits(cnt, 4);
      __builtin_amdgcn_s_setprio(2);
      const f32x4 z4 = {};
      bf16x8 t2f[8], xf[8], zf[2], yf[2];
#pragma unroll
      for (int f = 0; f < 8; ++f) xf[f] = ldsr(L + L_X + phi(f >> 2, l16) * 256 + (((g ^ l16) << 4) ^ ((f & 3) << 6)));
#pragma unroll
      for (int f = 0; f < 8; ++f) t2f[f] = ldsr(L + L_T2 + f * 1024 + v16);
      const LDSP unsigned* wt = (const LDSP unsigned*)(L + L_EX + w4 * 2048 + g * 512) + l16;
      unsigned wq[8];
#pragma unroll
      for (int e = 0; e < 8; ++e) wq[e] = wt[16 * e];
      f32x4 zr[2] = {z4, z4}, zi[2] = {z4, z4};
#pragma unroll
      for (int wp = 0; wp < 4; ++wp)
#pragma unroll
        for (int rt = 0; rt < 2; ++rt) { zr[rt] = mfma16(xf[rt * 4 + wp], t1[2 * wp], zr[rt]); zi[rt] = mfma16(xf[rt * 4 + wp], t1[1 + 2 * wp], zi[rt]); }
#pragma unroll
      for (int rt = 0; rt < 2; ++rt) { const f32x4 c = tw[2 * rt], sn = tw[2 * rt + 1]; zf[rt] = asb(pack44(zr[rt] * c + zi[rt] * sn, zi[rt] * c - zr[rt] * sn)); }
#pragma unroll
      for (int ta = 0; ta < 2; ++ta) {
        f32x4 yr = mfma16(t2f[4 * ta], zf[0], z4), yi = mfma16(t2f[4 * ta + 1], zf[0], z4);
        yr = mfma16(t2f[4 * ta + 2], zf[1], yr); yi = mfma16(t2f[4 * ta + 3], zf[1], yi);
        const f32x4 c = {bflo(wq[4 * ta]), bflo(wq[4 * ta + 1]), bflo(wq[4 * ta + 2]), bflo(wq[4 * ta + 3])};
        const f32x4 sn = {bfhi(wq[4 * ta]), bfhi(wq[4 * ta + 1]), bfhi(wq[4 * ta + 2]), bfhi(wq[4 * ta + 3])};
        yf[ta] = asb(pack44(yr * sn + yi * c, yr * c - yi * sn));
      }
#pragma unroll
      for (int rt = 0; rt < 2; ++rt) {
        f32x4 ur = mfma16(yf[0], t2f[4 * rt + 1], z4), ui = mfma16(yf[0], t2f[4 * rt], z4);
        ur = mfma16(yf[1], t2f[4 * rt + 3], ur); ui = mfma16(yf[1], t2f[4 * rt + 2], ui);
        const f32x4 c = tw[4 + 2 * rt], sn = tw[5 + 2 * rt];
        *((LDSP u32x4*)(L + L_EX + (w4 * 2 + rt) * 1024) + lane) = pack44(ui * c + ur * sn, ur * c - ui * sn);
      }
      lds_inc(lds0 + L_CNT + 4);
      lds_wait(cnt + 1, 4);
      f32x4 orr[2] = {z4, z4}, oi[2] = {z4, z4};
#pragma unroll
      for (int f = 0; f < 8; ++f) xf[f] = ldsr(L + L_EX + f * 1024 + v16);
#pragma unroll
      for (int wp = 0; wp < 4; ++wp)
#pragma unroll
        for (int rt = 0; rt < 2; ++rt) { orr[rt] = mfma16(xf[wp * 2 + rt], t1[1 + 2 * wp], orr[rt]); oi[rt] = mfma16(xf[wp * 2 + rt], t1[2 * wp], oi[rt]); }
      if (dofft) {
        const int pre = (kd & 1) ? 223 + kd : (kd == 224 ? 1 : kd);
        const unsigned yo = WS_Y1 + pre * 4096 + (16 * w4 + l16) * 64 + g * 16;
        stc(rs, yo, pack44(orr[0], orr[1]));
        if (kd != 0 && kd != 224) stc(rs, yo + 4096, pack44(oi[0], oi[1]));
      }
      WAITV(0);
      if (lds_arrive(cnt + 6) == 3 && lane == 0 && dofft) stc1(rs, FL_Y + kd * 4, tag);
    }
    const int trow = 8 * w4 + (lane >> 3);
    const unsigned tsw = ((lane & 7) ^ (((trow >> 1) & 1) << 2)) << 4;
#pragma unroll 1
    for (int t = 0; t < 14; ++t) dma1((const char*)wo + t * (32 * 896), (unsigned)(trow * 896 + nb * 128) + tsw, lds0 + L_WO + t * 4096 + w4 * 1024);
    if (dotail) {
      __builtin_amdgcn_s_setprio(0);
      WAITV(0);
      lds_inc(lds0 + L_CNT + 44);
      lds_waits(cnt + 11, 4);
      __builtin_amdgcn_s_setprio(2);
      bf16x8 wf[32];
      {
        const int cn = 32 * blk + 8 * p;
        const LDSP char* wa = L + L_WO + ((w4 & 1) * 224 + 4 * h + q) * 128 + (cn & 8);
        const unsigned s0 = (((cn >> 4) ^ ((q >> 1) << 2)) & 7) << 4;
#pragma unroll
        for (int i = 0; i < 32; ++i) {
          const int ro_ = ((i >> 2) * 28 + ((i >> 1) & 1) * 16) * 128 + (int)((i & 1) ? (s0 ^ 64) : s0);
          wf[i] = cat(ldtr(wa + ro_), ldtr(wa + ro_ + ((i >= 30) ? (h ? 512 : 1024) : 1024)));
          PIN(wf[i]);
        }
      }
      lds_inc(lds0 + L_CNT + 8);
      __builtin_amdgcn_s_setprio(0);
      if (w == 4) {
        lds_waits(cnt + 3, 1);
#pragma unroll 1
        for (int spin = 0; spin < SPIN_CAP; ++spin) {
          dma1c(ws + FL_Y, v16, lds0 + L_FL);
          WAITV(0);
          const u32x4 f = *(const volatile LDSP u32x4*)(L + L_FL + v16);
          if (__all((4 * lane >= NF || f[0] == tag) && (4 * lane + 1 >= NF || f[1] == tag) && (4 * lane + 2 >= NF || f[2] == tag) && (4 * lane + 3 >= NF || f[3] == tag))) break;
          __builtin_amdgcn_s_sleep(8);
        }
        lds_inc(lds0 + L_CNT + 28);
      } else lds_waits(cnt + 7, 1);
      __builtin_amdgcn_s_setprio(2);
#pragma unroll 1
      for (int t = 0; t < 14; ++t) dma1c(ws + WS_Y1 + t * (32 * 4096), (unsigned)(trow * 4096 + rbk * 128) + tsw, lds0 + L_Y1 + t * 4096 + w4 * 1024);
      WAITV(0);
      lds_inc(lds0 + L_CNT + 32);
      __builtin_amdgcn_s_setprio(0);
      lds_waits(cnt + 4, 1);
      __builtin_amdgcn_s_setprio(2);
      lds_wait(cnt + 10, 4);
      const unsigned oo = WS_O + (unsigned)((rbk * 2 + (w4 >> 1)) * 32 + (w4 & 1) * 16) * 1024;
      u32x4 fx[16];
#pragma unroll
      for (int f = 0; f < 16; ++f) fx[f] = __builtin_amdgcn_raw_buffer_load_b128(rs, v16 + (f & 3) * 1024, oo + (f >> 2) * 4096, 16);
      f32x16 acc0 = {}, acc1 = {};
      int done = 16;
#pragma unroll
      for (int f = 0; f < 16; f += 2) {
        const unsigned m = max(max(max(fx[f][0], fx[f][1]), max(fx[f][2], fx[f][3])), max(max(fx[f + 1][0], fx[f + 1][1]), max(fx[f + 1][2], fx[f + 1][3])));
        if (done == 16) {
          if (__builtin_expect(__any(m == 0xffffffffu), 0)) done = f;
          else {
            acc0 = mfma32(wf[2 * f], asb(fx[f]), acc0); acc1 = mfma32(wf[2 * f + 1], asb(fx[f]), acc1);
            acc0 = mfma32(wf[2 * f + 2], asb(fx[f + 1]), acc0); acc1 = mfma32(wf[2 * f + 3], asb(fx[f + 1]), acc1);
          }
        }
      }
      if (__builtin_expect(done < 16, 0)) {
#pragma unroll 1
        for (int spin = 0; spin < SPIN_CAP; ++spin) {
          bool trust = false;
          if (spin >= 16) {
            const int hh = (w4 & 1) * 8 + (lane & 7), pj = (rbk * 2 + (w4 >> 1)) >> 2;
            trust = __all(ldc1(rs, FL_O + ((hh >> 1) + 8 * ((hh & 1) + 2 * pj)) * 4) == tag);
            asm volatile("" ::: "memory");
          }
          unsigned bad = 0;
#pragma unroll
          for (int f = 0; f < 16; ++f) {
            if (__any(max(max(fx[f][0], fx[f][1]), max(fx[f][2], fx[f][3])) == 0xffffffffu)) fx[f] = __builtin_amdgcn_raw_buffer_load_b128(rs, v16 + (f & 3) * 1024, oo + (f >> 2) * 4096, 16);
          }
#pragma unroll
          for (int f = 0; f < 16; ++f) bad = max(max(bad, max(fx[f][0], fx[f][1])), max(fx[f][2], fx[f][3]));
          if (trust || !__any(bad == 0xffffffffu)) break;
        }
#pragma unroll
        for (int f = 0; f < 16; f += 2) {
          if (f >= done) {
            acc0 = mfma32(wf[2 * f], asb(fx[f]), acc0); acc1 = mfma32(wf[2 * f + 1], asb(fx[f]), acc1);
            acc0 = mfma32(wf[2 * f + 2], asb(fx[f + 1]), acc0); acc1 = mfma32(wf[2 * f + 3], asb(fx[f + 1]), acc1);
          }
        }
      }
      const f32x16 snd = (w4 & 1) ? acc0 : acc1, kp = (w4 & 1) ? acc1 : acc0;
      LDSP f32x4* d = (LDSP f32x4*)(L + L_EX + w4 * 4096) + lane;
#pragma unroll
      for (int i = 0; i < 4; ++i) d[i * 64] = (f32x4){snd[4 * i], snd[4 * i + 1], snd[4 * i + 2], snd[4 * i + 3]};
      lds_inc(lds0 + L_CNT + 48 + (w4 >> 1) * 4);
      lds_wait(cnt + 12 + (w4 >> 1), 2);
      const LDSP f32x4* e2 = (const LDSP f32x4*)(L + L_EX + (w4 ^ 1) * 4096) + lane;
#pragma unroll
      for (int kh = 0; kh < 2; ++kh) {
        const f32x4 a = {kp[8 * kh], kp[8 * kh + 1], kp[8 * kh + 2], kp[8 * kh + 3]}, b = {kp[8 * kh + 4], kp[8 * kh + 5], kp[8 * kh + 6], kp[8 * kh + 7]};
        store_half(ro, rbk * 64 + (w4 >> 1) * 32, 448 + nb * 64 + (w4 & 1) * 32 + kh * 16, lane, e2[kh * 128] + a, e2[kh * 128 + 64] + b);
      }
    } else {
      WAITV(0);
      lds_inc(lds0 + L_CNT + 8);
    }
  }
}

extern "C" void kernel_launch(void* const* d_in, const int* in_sizes, int n_in, void* d_out, int out_size, void* d_ws, size_t ws_size,
                              hipStream_t stream) {
  k1<<<256, 512, 0, stream>>>((const u16*)d_in[0], (const u16*)d_in[1], (const u16*)d_in[2], (const u16*)d_in[3], (const u16*)d_in[4], (const u16*)d_in[5], (const u16*)d_out, (char*)d_ws);
  k2<<<256, 512, 0, stream>>>((const u16*)d_in[5], (u16*)d_out, (char*)d_ws);
}
```
